```python
import torch

def _softmax(x, axis=-1):
    return torch.softmax(x, dim=axis)
def _gelu(x):
    return torch.nn.functional.gelu(x, approximate="tanh")
def _silu(x):
    return torch.nn.functional.silu(x)
def _sigmoid(x):
    return torch.sigmoid(x)
def _layernorm(x, axis=-1, eps=1e-5):
    m = x.mean(dim=axis, keepdim=True)
    v = x.var(dim=axis, keepdim=True, unbiased=False)
    return (x - m) / torch.sqrt(v + eps)
def _scatter_add(tgt, idx, src, axis=0):
    return tgt.clone().index_add_(axis, idx, src)
def _segment_sum(src, seg, n):
    out = torch.zeros((n,) + src.shape[1:], dtype=src.dtype)
    return out.index_add_(0, seg, src)
def _pad(x, pw):
    flat = tuple(v for pair in reversed(pw) for v in pair)
    return torch.nn.functional.pad(x, flat)

kernel_name = 'standalone__deep_cross_wide_deep__recsys'

def setup_inputs():

    _w = (lambda a: (lambda t: t.to(torch.bfloat16) if t.is_floating_point() else t)(torch.as_tensor(a)))
    B, F, D = 1024, 100, 128
    V, Nd = min(1000, 100000), 13
    Din = F * D
    H = 4 * D
    return {
        "idx": _w((lambda lo, hi, *s: torch.randint(lo, hi, s))(0, V, B, F).to(torch.int64)),
        "table": _w((lambda *s: torch.randn(*s, dtype=torch.bfloat16) * 1.6)(V, D) * 0.05),
        "x_wide": _w((lambda *s: torch.randn(*s, dtype=torch.bfloat16) * 1.6)(B, Nd)),
        "w_wide": _w((lambda *s: torch.randn(*s, dtype=torch.bfloat16) * 1.6)(Nd) * 0.05),
        "b_wide": _w((lambda *s: torch.randn(*s, dtype=torch.bfloat16) * 1.6)(1) * 0.01),
        "W1": _w((lambda *s: torch.randn(*s, dtype=torch.bfloat16) * 1.6)(Din, H) * (2.0 / Din) ** 0.5),
        "b1": _w((lambda *s: torch.randn(*s, dtype=torch.bfloat16) * 1.6)(H) * 0.01),
        "W2": _w((lambda *s: torch.randn(*s, dtype=torch.bfloat16) * 1.6)(H) * (1.0 / H) ** 0.5),
        "b2": _w((lambda *s: torch.randn(*s, dtype=torch.bfloat16) * 1.6)(1) * 0.01),
    }

def reference(idx, table, x_wide, w_wide, b_wide, W1, b1, W2, b2):
    emb = table[idx]
    deep_in = emb.reshape(emb.shape[0], -1)
    h = deep_in @ W1 + b1
    h = h * (lambda x, dt: x.to(dt))(h > 0, h.dtype)
    deep_logit = h @ W2 + b2
    wide_logit = x_wide @ w_wide + b_wide
    return _sigmoid(wide_logit + deep_logit)
```

```cpp
#include <hip/hip_runtime.h>
#include <hip/hip_bf16.h>
#include <stdint.h>

#define XMAP 1

typedef __attribute__((ext_vector_type(8))) __bf16 bf16x8;
typedef __attribute__((ext_vector_type(4))) float f32x4;
typedef __attribute__((ext_vector_type(4))) unsigned u32x4;
typedef __attribute__((ext_vector_type(2))) unsigned u32x2;
typedef unsigned long long u64;
typedef __attribute__((address_space(1))) u64 gu64;
typedef __attribute__((address_space(3))) u32x4 lds_u32x4;
typedef __attribute__((address_space(3))) f32x4 lds_f32x4;
typedef __attribute__((address_space(3))) void lds_void;

constexpr int kB = 1024, kF = 100, kD = 128, kV = 1000, kND = 13, kH = 512;
constexpr int TROW = 80;
constexpr int IDX_STRIDE = 1040;
constexpr int NSTEP = 25;
constexpr size_t WS_EPOCH = 0;
constexpr size_t WS_FLAG = 4096;
constexpr size_t WS_X = 65536;
#define STAMP(k) do {} while (0)

__device__ __forceinline__ float bf2f(unsigned short h) { return __uint_as_float((unsigned)h << 16); }
__device__ __forceinline__ float rbf(float x) {
  return (float)(__bf16)x;
}
__device__ __forceinline__ unsigned mad_lo(unsigned p, unsigned mul, unsigned add) {
  unsigned r; asm("v_mad_u32_u16 %0, %1, %2, %3" : "=v"(r) : "v"(p), "s"(mul), "v"(add)); return r; }
__device__ __forceinline__ unsigned mad_hi(unsigned p, unsigned mul, unsigned add) {
  unsigned r; asm("v_mad_u32_u16 %0, %1, %2, %3 op_sel:[1,0,0,0]" : "=v"(r) : "v"(p), "s"(mul), "v"(add)); return r; }

__global__ __launch_bounds__(256, 1) void wd_fused(
    const int* __restrict__ idx, const unsigned short* __restrict__ table,
    const unsigned short* __restrict__ xw, const unsigned short* __restrict__ ww,
    const unsigned short* __restrict__ bw, const unsigned short* __restrict__ W1,
    const unsigned short* __restrict__ b1, const unsigned short* __restrict__ W2,
    const unsigned short* __restrict__ b2, unsigned short* __restrict__ out, char* ws) {
  __shared__ __attribute__((aligned(16))) char s_tab[98304];
  __shared__ __attribute__((aligned(16))) char s_idx[NSTEP * IDX_STRIDE + 2 * IDX_STRIDE];

  const int tid = threadIdx.x, l = tid & 63;
  const int w = __builtin_amdgcn_readfirstlane(tid >> 6);
  const int bid = blockIdx.x;
  const int xc = bid & 7, r5 = bid >> 3;
  const int s = xc >> 1, nb = (xc & 1) * 4 + (r5 & 3), m = r5 >> 2;
  const int prod = s * 8 + nb;
  const int g = l >> 4, c = l & 15;
  STAMP(0);

  const u64 epoch = __hip_atomic_load((gu64*)(ws + WS_EPOCH + m * 256), __ATOMIC_RELAXED, __HIP_MEMORY_SCOPE_AGENT);

  const __amdgpu_buffer_rsrc_t rs_idx = __builtin_amdgcn_make_buffer_rsrc((void*)(idx + m * 128 * kF), 0, 128 * kF * 4, 0x00020000);
  const int q0 = w * 100 + l, q1 = w * 100 + 64 + l;
  u32x4 xa[8], xb[8];
#pragma unroll
  for (int i = 0; i < 8; ++i) xa[i] = __builtin_amdgcn_raw_buffer_load_b128(rs_idx, q0 * 16, i * 6400, 0);
  {
    const int vo = l < 36 ? q1 * 16 : 0x40000000;
#pragma unroll
    for (int i = 0; i < 8; ++i) xb[i] = __builtin_amdgcn_raw_buffer_load_b128(rs_idx, vo, i * 6400, 0);
  }

  const __amdgpu_buffer_rsrc_t rs_w1 = __builtin_amdgcn_make_buffer_rsrc((void*)W1, 0, kF * kD * kH * 2, 0x00020000);
  const int vb0 = g * 131072 + (32 * s + 8 * w) * 1024 + nb * 128 + c * 8;
  const int vb1 = vb0 + 4096;
  u32x2 ring[5][8];
#define LOADB(slot, e, j) ring[slot][e] = __builtin_amdgcn_raw_buffer_load_b64(rs_w1, ((e) < 4 ? vb0 : vb1) + ((e) & 3) * 1024, (j) * 524288, 0)
#pragma unroll
  for (int e = 0; e < 8; ++e) LOADB(0, e, 0);

  {
    const __amdgpu_buffer_rsrc_t rs_tab = __builtin_amdgcn_make_buffer_rsrc((void*)table, 0, kV * kD * 2, 0x00020000);
    int vt[5];
#pragma unroll
    for (int b = 0; b < 5; ++b) {
      const int sl = b * 64 + l, v = sl / 5, ch = sl - v * 5;
      vt[b] = ch < 4 ? v * 256 + ch * 16 + s * 64 : 0x40000000;
    }
#pragma unroll
    for (int a = 0; a < 4; ++a)
#pragma unroll
      for (int b = 0; b < 5; ++b)
        __builtin_amdgcn_raw_ptr_buffer_load_lds(rs_tab, (lds_void*)(s_tab + (w * 20 + a * 5 + b) * 1024), 16, vt[b], (w * 4 + a) * 16384, 0, 0);
  }
#pragma unroll
  for (int u = 1; u < 5; ++u)
#pragma unroll
    for (int e = 0; e < 8; ++e) LOADB(u, e, u);

  {
    const int r0 = q0 / 25, j0 = q0 - r0 * 25;
    char* p0 = s_idx + j0 * IDX_STRIDE + r0 * 16;
#pragma unroll
    for (int gg = 0; gg < 4; ++gg) {
      u32x4 v;
#pragma unroll
      for (int p = 0; p < 4; ++p) v[p] = xa[2 * p][gg] | (xa[2 * p + 1][gg] << 16);
      *(u32x4*)(p0 + gg * 256) = v;
    }
    if (l < 36) {
      const int r1 = q1 / 25, j1 = q1 - r1 * 25;
      char* p1 = s_idx + j1 * IDX_STRIDE + r1 * 16;
#pragma unroll
      for (int gg = 0; gg < 4; ++gg) {
        u32x4 v;
#pragma unroll
        for (int p = 0; p < 4; ++p) v[p] = xb[2 * p][gg] | (xb[2 * p + 1][gg] << 16);
        *(u32x4*)(p1 + gg * 256) = v;
      }
    }
  }
  STAMP(1);
  asm volatile("s_waitcnt vmcnt(32) lgkmcnt(0)" ::: "memory");
  __builtin_amdgcn_s_barrier();
  asm volatile("" ::: "memory");
  STAMP(2);

  f32x4 acc[8][4];
#pragma unroll
  for (int i = 0; i < 8; ++i)
#pragma unroll
    for (int t = 0; t < 4; ++t) acc[i][t] = f32x4{0.f, 0.f, 0.f, 0.f};

  const unsigned abase = (unsigned)(uintptr_t)(lds_void*)s_tab + 16u * w;
  const char* pidx = s_idx + l * 16;
  u32x4 A[8], Bf[4], Bn[4];
  u32x4 ix = *(const u32x4*)(pidx), ixn;
#define GATHER(i, iv) A[i] = *(const lds_u32x4*)(uintptr_t)(((i) & 1) ? mad_hi(iv[(i) >> 1], TROW, abase) : mad_lo(iv[(i) >> 1], TROW, abase))
#define PERMB(dst, slot, p) do { \
    dst[0][p] = __builtin_amdgcn_perm(ring[slot][2 * (p) + 1][0], ring[slot][2 * (p)][0], 0x05040100u); \
    dst[1][p] = __builtin_amdgcn_perm(ring[slot][2 * (p) + 1][0], ring[slot][2 * (p)][0], 0x07060302u); \
    dst[2][p] = __builtin_amdgcn_perm(ring[slot][2 * (p) + 1][1], ring[slot][2 * (p)][1], 0x05040100u); \
    dst[3][p] = __builtin_amdgcn_perm(ring[slot][2 * (p) + 1][1], ring[slot][2 * (p)][1], 0x07060302u); } while (0)
#pragma unroll
  for (int i = 0; i < 8; ++i) GATHER(i, ix);
  ix = *(const u32x4*)(pidx + IDX_STRIDE);
#pragma unroll
  for (int p = 0; p < 4; ++p) PERMB(Bf, 0, p);

  for (int jo = 0; jo < 5; ++jo) {
#pragma unroll
    for (int u = 0; u < 5; ++u) {
      const int j = jo * 5 + u;
      ixn = *(const u32x4*)(pidx + (j + 2) * IDX_STRIDE);
#pragma unroll
      for (int i = 0; i < 8; ++i) {
#pragma unroll
        for (int t = 0; t < 4; ++t)
          acc[i][t] = __builtin_amdgcn_mfma_f32_16x16x32_bf16(__builtin_bit_cast(bf16x8, A[i]), __builtin_bit_cast(bf16x8, Bf[t]), acc[i][t], 0, 0, 0);
        GATHER(i, ix);
        LOADB(u, i, j + 5);
        if (i & 1) PERMB(Bn, (u + 1) % 5, i >> 1);
      }
#pragma unroll
      for (int t = 0; t < 4; ++t) Bf[t] = Bn[t];
      ix = ixn;
    }
  }
  STAMP(3);

  const int colA = w * 64 + 4 * c + g, colB = colA + 256;
  const float b1A = bf2f(b1[colA]), b1B = bf2f(b1[colB]), w2A = bf2f(W2[colA]), w2B = bf2f(W2[colB]);
  const int row0 = m * 128 + prod * 4;
  float wide = 0.f;
  if (w == 0) {
    float p = c < kND ? bf2f(xw[(row0 + g) * kND + c]) * bf2f(ww[c]) : 0.f;
    p += __shfl_xor(p, 1); p += __shfl_xor(p, 2); p += __shfl_xor(p, 4); p += __shfl_xor(p, 8);
    wide = rbf(rbf(p) + bf2f(bw[0]));
  }

  __builtin_amdgcn_s_barrier();
  asm volatile("" ::: "memory");
  f32x4 own[8];
#pragma unroll
  for (int o = 0; o < 4; ++o) {
    if (o == w) {
#pragma unroll
      for (int k = 0; k < 8; ++k) own[k] = acc[2 * o + (k >> 2)][k & 3];
    } else {
      char* p = s_tab + (o * 3 + ((w - o - 1) & 3)) * 8192 + l * 16;
#pragma unroll
      for (int k = 0; k < 8; ++k) *(f32x4*)(p + k * 1024) = acc[2 * o + (k >> 2)][k & 3];
    }
  }
  asm volatile("s_waitcnt lgkmcnt(0)" ::: "memory");
  __builtin_amdgcn_s_barrier();
  asm volatile("" ::: "memory");
  {
    const char* p = s_tab + w * 3 * 8192 + l * 16;
#pragma unroll
    for (int k = 0; k < 8; ++k)
#pragma unroll
      for (int q = 0; q < 3; ++q) own[k] += *(const f32x4*)(p + q * 8192 + k * 1024);
  }
  STAMP(4);

  const __amdgpu_buffer_rsrc_t rs_x = __builtin_amdgcn_make_buffer_rsrc((void*)(ws + WS_X + (size_t)m * 1048576), 0, 1048576, 0x00020000);
  {
    const int vo = (8 * w + g) * 32768 + prod * 1024 + c * 16;
#pragma unroll
    for (int k = 0; k < 8; ++k)
      __builtin_amdgcn_raw_buffer_store_b128(__builtin_bit_cast(u32x4, own[k]), rs_x, vo + (k & 3) * 256, (k >> 2) * 131072, 16);
  }
  const u64 tag = epoch * 6364136223846793005ull + 1442695040888963407ull;
  asm volatile("s_waitcnt vmcnt(0)" ::: "memory");
  __builtin_amdgcn_s_barrier();
  asm volatile("" ::: "memory");
  gu64* flags = (gu64*)(ws + WS_FLAG + m * 32 * 64);
  if (tid == 0) __hip_atomic_store(flags + prod * 8, tag, __ATOMIC_RELAXED, __HIP_MEMORY_SCOPE_AGENT);
  STAMP(5);

  {
    gu64* fp = flags + ((l & 3) * 8 + w + (l & 4)) * 8;
    for (unsigned spins = 0; spins < (1u << 22); ++spins) {
      const u64 v = __hip_atomic_load(fp, __ATOMIC_RELAXED, __HIP_MEMORY_SCOPE_AGENT);
      if (__all(v == tag)) break;
      __builtin_amdgcn_s_sleep(1);
    }
    __builtin_amdgcn_fence(__ATOMIC_ACQUIRE, "wavefront");
    asm volatile("" ::: "memory");
  }
  STAMP(6);
  f32x4 hA, hB;
  {
    const int vo = prod * 32768 + w * 1024 + l * 16;
    u32x4 pa[4], pb[4];
#pragma unroll
    for (int ss = 0; ss < 4; ++ss) {
      pa[ss] = __builtin_amdgcn_raw_buffer_load_b128(rs_x, vo, ss * 8192, 16);
      pb[ss] = __builtin_amdgcn_raw_buffer_load_b128(rs_x, vo, ss * 8192 + 4096, 16);
    }
    hA = (__builtin_bit_cast(f32x4, pa[0]) + __builtin_bit_cast(f32x4, pa[1])) + (__builtin_bit_cast(f32x4, pa[2]) + __builtin_bit_cast(f32x4, pa[3]));
    hB = (__builtin_bit_cast(f32x4, pb[0]) + __builtin_bit_cast(f32x4, pb[1])) + (__builtin_bit_cast(f32x4, pb[2]) + __builtin_bit_cast(f32x4, pb[3]));
  }
  f32x4 dp;
#pragma unroll
  for (int r = 0; r < 4; ++r) {
    float a = rbf(rbf(hA[r]) + b1A), b = rbf(rbf(hB[r]) + b1B);
    a = a > 0.f ? a : a * 0.f;
    b = b > 0.f ? b : b * 0.f;
    dp[r] = a * w2A + b * w2B;
  }
  float v2[2], v1;
  {
    const bool up = l & 32;
    const float s0 = up ? dp[0] : dp[2], s1 = up ? dp[1] : dp[3];
    const float k0 = up ? dp[2] : dp[0], k1 = up ? dp[3] : dp[1];
    v2[0] = k0 + __shfl_xor(s0, 32); v2[1] = k1 + __shfl_xor(s1, 32);
    const bool up2 = l & 16;
    const float sx = up2 ? v2[0] : v2[1], kx = up2 ? v2[1] : v2[0];
    v1 = kx + __shfl_xor(sx, 16);
    v1 += __shfl_xor(v1, 1); v1 += __shfl_xor(v1, 2); v1 += __shfl_xor(v1, 4); v1 += __shfl_xor(v1, 8);
  }
  float* s_fin = (float*)s_idx;
  if (c == 0) s_fin[w * 4 + g] = v1;
  __syncthreads();
  if (w == 0) {
    if (c == 0) {
      const float d = (s_fin[g] + s_fin[4 + g]) + (s_fin[8 + g] + s_fin[12 + g]);
      const float deep = rbf(rbf(d) + bf2f(b2[0]));
      const float z = rbf(wide + deep);
      const float y = 1.f / (1.f + __expf(-z));
      out[row0 + g] = __builtin_bit_cast(unsigned short, (__bf16)y);
    }
    if (prod == 0 && l == 0)
      __hip_atomic_store((gu64*)(ws + WS_EPOCH + m * 256), tag, __ATOMIC_RELAXED, __HIP_MEMORY_SCOPE_AGENT);
  }
  STAMP(7);
}

extern "C" void kernel_launch(void* const* d_in, const int* in_sizes, int n_in,
                              void* d_out, int out_size, void* d_ws, size_t ws_size,
                              hipStream_t stream) {
  (void)in_sizes; (void)n_in; (void)out_size; (void)ws_size;
  wd_fused<<<256, 256, 0, stream>>>(
      (const int*)d_in[0], (const unsigned short*)d_in[1], (const unsigned short*)d_in[2],
      (const unsigned short*)d_in[3], (const unsigned short*)d_in[4], (const unsigned short*)d_in[5],
      (const unsigned short*)d_in[6], (const unsigned short*)d_in[7], (const unsigned short*)d_in[8],
      (unsigned short*)d_out, (char*)d_ws);
}
```

```cpp
#include <hip/hip_runtime.h>
#include <hip/hip_bf16.h>
#include <stdint.h>

typedef __attribute__((ext_vector_type(4))) unsigned u32x4;
typedef __attribute__((ext_vector_type(8))) unsigned u32x8;
typedef __attribute__((ext_vector_type(16))) unsigned u32x16;
typedef unsigned long long u64;
typedef __attribute__((address_space(1))) u64 gu64;
typedef __attribute__((address_space(1))) unsigned short gu16;

constexpr int ROT = 4;
constexpr int kF = 100, kD = 128, kV = 1000, kND = 13, kH = 512;
constexpr unsigned X_FLAG = 524288, X_EPOCH = 540672, X_BYTES = 544768;

__device__ __forceinline__ float lo2f(unsigned h) { return __uint_as_float(h << 16); }
__device__ __forceinline__ float hi2f(unsigned h) { return __uint_as_float(h & 0xffff0000u); }
__device__ __forceinline__ float rbf(float x) { return (float)(__bf16)x; }

typedef __attribute__((ext_vector_type(2))) float f32x2;
typedef __attribute__((ext_vector_type(2))) __bf16 bf16x2;
__device__ __forceinline__ f32x2 up(unsigned u) { f32x2 r; r[0] = lo2f(u); r[1] = hi2f(u); return r; }
__device__ __forceinline__ unsigned pk(f32x2 x) { return __builtin_bit_cast(unsigned, __builtin_convertvector(x, bf16x2)); }
template <int C> __device__ __forceinline__ float dpp_add(float x) {
  return x + __int_as_float(__builtin_amdgcn_update_dpp(0, __float_as_int(x), C, 0xf, 0xf, true));
}
__device__ __forceinline__ float row_sum(float x) {
  return dpp_add<0x121>(dpp_add<0x122>(dpp_add<0x124>(dpp_add<0x128>(x))));
}

__global__ __launch_bounds__(256, 1) void wd_fused(
    const int* __restrict__ idx, const unsigned short* __restrict__ table,
    const unsigned short* __restrict__ xw, const unsigned short* __restrict__ ww,
    const unsigned short* __restrict__ bw, const unsigned short* __restrict__ W1,
    const unsigned short* __restrict__ b1, const unsigned short* __restrict__ W2,
    const unsigned short* __restrict__ b2, unsigned short* __restrict__ out, char* ws) {
  __shared__ __attribute__((aligned(16))) char smem[163840];

  int tid = threadIdx.x, bid = blockIdx.x;
  u32x16 rs, sp;
  asm volatile(
      "s_load_dwordx2 s[68:69], %4, 0x28\n s_load_dwordx2 s[64:65], %4, 0x0\n s_load_dwordx2 s[72:73], %4, 0x8\n"
      "s_load_dwordx2 s[76:77], %4, 0x50\n s_load_dwordx16 s[80:95], %4, 0x10\n"
      "s_getpc_b64 s[40:41]\n .Lwd_code_beg:\n s_lshr_b32 s43, %3, 3\n s_lshl_b32 s43, s43, 7\n"
      "v_lshl_add_u32 v253, %2, 12, s43\n s_and_b32 s41, s41, 0xffff\n"
      "s_mov_b32 s42, .Lwd_code_end-.Lwd_code_beg\n s_mov_b32 s43, 0x20000\n"
      "buffer_load_dword a236, v253, s[40:43], 0 offen\n"
      "s_mov_b32 s66, 51200\n s_mov_b32 s67, 0x20000\n s_mov_b32 s70, 13107200\n s_mov_b32 s71, 0x20000\n"
      "s_mov_b32 s74, 256000\n s_mov_b32 s75, 0x20000\n s_mov_b32 s78, 544768\n s_mov_b32 s79, 0x20000\n"
      : "=&{s[64:79]}"(rs), "=&{s[80:95]}"(sp), "+v"(tid), "+s"(bid)
      : "s"(__builtin_amdgcn_kernarg_segment_ptr())
      : "scc", "s40", "s41", "s42", "s43", "v253", "a236");
  const int l = tid & 63;
  const int w = __builtin_amdgcn_readfirstlane(tid >> 6);
  const int xc = bid & 7, r5 = bid >> 3;
  const int s = xc >> 1, nb = (xc & 1) * 4 + (r5 & 3), m = r5 >> 2;
  const int prod = s * 8 + nb;
  const int g = l >> 4, c = l & 15, h = c >> 3, kq = c & 7;
  u32x16 vin;
  u32x8 sm;
  u32x4 sq, sr;
  sq[0] = w; sq[1] = (w * 16 + r5 * 2) & 63; sq[2] = ((r5 & 3) * 4 + w) * 128; sq[3] = (r5 * 4 + w) * 256 + 64 * s;
  sr[0] = m * (128 * kF * 4); sr[1] = m * 1048576; sr[2] = w * 1024; sr[3] = 0;
  const int row = m * 128 + prod * 4 + w;
  vin[0] = l * 16;
  vin[1] = (l >> 2) * 256 + (l & 3) * 16 + 64 * s;
  vin[2] = (4 * w + g) * 131072 + (32 * s + 8 * h) * 1024 + nb * 128 + kq * 16;
  vin[3] = (96 + w) * 131072 + (32 * s + 8 * g) * 1024 + nb * 128 + kq * 16 + 8 * h;
#pragma unroll
  for (int q = 0; q < 4; ++q) vin[4 + q] = 65536 + (16 * ((q + w + 1) & 3) + c) * 400 + 4 * (4 * w + g);
  vin[8] = 4 * (96 + w) - 4 * (4 * w + g);
  vin[9] = 16 * g;
#pragma unroll
  for (int q = 0; q < 3; ++q) vin[10 + q] = 65536 + ((q + w + 1) & 3) * 12288 + l * 16;
  vin[13] = 65536 + w * 12288 + l * 16;
  const int slot = (16 * w + 4 * g) * 4096 + ((prod + ROT * (4 * w + g)) & 31) * 128;
  vin[14] = slot + c * 8;
  const unsigned vfl = X_FLAG + (((prod >> 2) & 3) * 32 + (l & 31)) * 64;
  vin[15] = vfl;
  sm[0] = ((l >> 3) * 64 + 16 * (l & 3) + 4 * ((l >> 2) & 1)) * 2;
  sm[2] = 2 * (c < kND ? c : kND - 1);
  sm[1] = row * (2 * kND) + sm[2];
  sm[3] = sm[4] = 0;
  sm[5] = slot + h * 4096 + kq * 16;
  sm[7] = (0 + 16 * g + 4 * w + (c >> 2)) * 131072 + (32 * s + r5) * 1024 + ((xc & 1) * 4 + (l & 3)) * 128;
  sm[6] = X_FLAG + (w * 32 + prod) * 64;
  u32x4 fo;
  u32x8 bw2;
  asm volatile(
      "v_lshl_add_u32 v255, v8, 7, s98\n"
      "v_lshl_add_u32 v254, v8, 11, s99\n"
      "v_add_u32 v247, 12582912, v31\n"
      "s_mov_b32 s42, 0x05040100\n"
      "s_mov_b32 s43, 0x07060302\n"
      "s_movk_i32 s46, 65\n"
      "s_mov_b32 vcc_lo, 0x00ff00ff\n"
      "s_mov_b32 vcc_hi, 0x00ff00ff\n"
      "s_mov_b32 s47, 540672\n"
      "s_mov_b32 s44, s38\n"
      "s_waitcnt lgkmcnt(0)\n"
      "s_add_u32 s64, s64, s36\n"
      "s_addc_u32 s65, s65, 0\n"
      "buffer_load_dword a236, v255, s[64:67], 0 offen\n"
      "buffer_load_dword a236, v254, s[72:75], 0 offen\n"
      "buffer_load_dword a236, v247, s[68:71], 0 offen\n"
      "s_mov_b32 exec_lo, 0xffffffff\n"
      "s_mov_b32 exec_hi, 0x0\n"
      "buffer_load_dword a236, v31, s[68:71], 0 offen\n"
      "s_mov_b64 exec, -1\n"
      "s_add_u32 s76, s76, s37\n"
      "s_addc_u32 s77, s77, 0\n"
      "s_add_u32 m0, s44, 65536\n"
      "s_nop 0\n"
      "buffer_load_dwordx4 v8, s[64:67], s44 offen lds\n"
      "s_add_u32 s44, s44, 4096\n"
      "s_add_u32 m0, s44, 65536\n"
      "s_nop 0\n"
      "buffer_load_dwordx4 v8, s[64:67], s44 offen lds\n"
      "s_add_u32 s44, s44, 4096\n"
      "s_add_u32 m0, s44, 65536\n"
      "s_nop 0\n"
      "buffer_load_dwordx4 v8, s[64:67], s44 offen lds\n"
      "s_add_u32 s44, s44, 4096\n"
      "s_add_u32 m0, s44, 65536\n"
      "s_nop 0\n"
      "buffer_load_dwordx4 v8, s[64:67], s44 offen lds\n"
      "s_add_u32 s44, s44, 4096\n"
      "s_add_u32 m0, s44, 65536\n"
      "s_nop 0\n"
      "buffer_load_dwordx4 v8, s[64:67], s44 offen lds\n"
      "s_add_u32 s44, s44, 4096\n"
      "s_add_u32 m0, s44, 65536\n"
      "s_nop 0\n"
      "buffer_load_dwordx4 v8, s[64:67], s44 offen lds\n"
      "s_add_u32 s44, s44, 4096\n"
      "s_add_u32 m0, s44, 65536\n"
      "s_nop 0\n"
      "buffer_load_dwordx4 v8, s[64:67], s44 offen lds\n"
      "s_add_u32 s44, s44, 4096\n"
      "s_add_u32 m0, s44, 65536\n"
      "s_nop 0\n"
      "buffer_load_dwordx4 v8, s[64:67], s44 offen lds\n"
      "s_add_u32 s44, s44, 4096\n"
      "s_add_u32 m0, s44, 65536\n"
      "s_nop 0\n"
      "buffer_load_dwordx4 v8, s[64:67], s44 offen lds\n"
      "s_add_u32 s44, s44, 4096\n"
      "s_add_u32 m0, s44, 65536\n"
      "s_nop 0\n"
      "buffer_load_dwordx4 v8, s[64:67], s44 offen lds\n"
      "s_add_u32 s44, s44, 4096\n"
      "s_add_u32 m0, s44, 65536\n"
      "s_nop 0\n"
      "buffer_load_dwordx4 v8, s[64:67], s44 offen lds\n"
      "s_add_u32 s44, s44, 4096\n"
      "s_add_u32 m0, s44, 65536\n"
      "s_nop 0\n"
      "buffer_load_dwordx4 v8, s[64:67], s44 offen lds\n"
      "s_add_u32 s44, s44, 4096\n"
      "s_min_u32 s44, s44, 50176\n"
      "s_add_u32 m0, s44, 65536\n"
      "s_nop 0\n"
      "buffer_load_dwordx4 v8, s[64:67], s44 offen lds\n"
      "s_mov_b32 s40, 0\n"
      "buffer_load_dwordx2 v[96:97], v11, s[68:71], s40 offen\n"
      "buffer_load_dwordx2 v[100:101], v11, s[68:71], s40 offen offset:1024\n"
      "buffer_load_dwordx2 v[104:105], v11, s[68:71], s40 offen offset:2048\n"
      "buffer_load_dwordx2 v[108:109], v11, s[68:71], s40 offen offset:3072\n"
      "s_mov_b32 s40, 4096\n"
      "buffer_load_dwordx2 v[112:113], v11, s[68:71], s40 offen\n"
      "buffer_load_dwordx2 v[116:117], v11, s[68:71], s40 offen offset:1024\n"
      "buffer_load_dwordx2 v[120:121], v11, s[68:71], s40 offen offset:2048\n"
      "buffer_load_dwordx2 v[124:125], v11, s[68:71], s40 offen offset:3072\n"
      "s_mov_b32 s40, 0\n"
      "buffer_load_dwordx4 v[128:131], v10, s[68:71], s40 offen\n"
      "buffer_load_dwordx4 v[132:135], v10, s[68:71], s40 offen offset:1024\n"
      "buffer_load_dwordx4 v[136:139], v10, s[68:71], s40 offen offset:2048\n"
      "buffer_load_dwordx4 v[140:143], v10, s[68:71], s40 offen offset:3072\n"
      "s_mov_b32 s40, 4096\n"
      "buffer_load_dwordx4 v[144:147], v10, s[68:71], s40 offen\n"
      "buffer_load_dwordx4 v[148:151], v10, s[68:71], s40 offen offset:1024\n"
      "buffer_load_dwordx4 v[152:155], v10, s[68:71], s40 offen offset:2048\n"
      "buffer_load_dwordx4 v[156:159], v10, s[68:71], s40 offen offset:3072\n"
      "s_mov_b32 s41, s97\n"
      "s_min_u32 s45, s41, 62\n"
      "s_mul_i32 m0, s45, 1040\n"
      "v_lshl_add_u32 v248, s45, 12, v9\n"
      "s_add_u32 s41, s41, 1\n"
      "buffer_load_dwordx4 v248, s[72:75], 0 offen lds\n"
      "s_and_b32 s41, s41, 63\n"
      "s_min_u32 s45, s41, 62\n"
      "s_mul_i32 m0, s45, 1040\n"
      "v_lshl_add_u32 v249, s45, 12, v9\n"
      "s_add_u32 s41, s41, 1\n"
      "buffer_load_dwordx4 v249, s[72:75], 0 offen lds\n"
      "s_and_b32 s41, s41, 63\n"
      "s_min_u32 s45, s41, 62\n"
      "s_mul_i32 m0, s45, 1040\n"
      "v_lshl_add_u32 v248, s45, 12, v9\n"
      "s_add_u32 s41, s41, 1\n"
      "buffer_load_dwordx4 v248, s[72:75], 0 offen lds\n"
      "s_and_b32 s41, s41, 63\n"
      "s_min_u32 s45, s41, 62\n"
      "s_mul_i32 m0, s45, 1040\n"
      "v_lshl_add_u32 v249, s45, 12, v9\n"
      "s_add_u32 s41, s41, 1\n"
      "buffer_load_dwordx4 v249, s[72:75], 0 offen lds\n"
      "s_and_b32 s41, s41, 63\n"
      "s_min_u32 s45, s41, 62\n"
      "s_mul_i32 m0, s45, 1040\n"
      "v_lshl_add_u32 v248, s45, 12, v9\n"
      "s_add_u32 s41, s41, 1\n"
      "buffer_load_dwordx4 v248, s[72:75], 0 offen lds\n"
      "s_and_b32 s41, s41, 63\n"
      "s_min_u32 s45, s41, 62\n"
      "s_mul_i32 m0, s45, 1040\n"
      "v_lshl_add_u32 v249, s45, 12, v9\n"
      "s_add_u32 s41, s41, 1\n"
      "buffer_load_dwordx4 v249, s[72:75], 0 offen lds\n"
      "s_and_b32 s41, s41, 63\n"
      "s_min_u32 s45, s41, 62\n"
      "s_mul_i32 m0, s45, 1040\n"
      "v_lshl_add_u32 v248, s45, 12, v9\n"
      "s_add_u32 s41, s41, 1\n"
      "buffer_load_dwordx4 v248, s[72:75], 0 offen lds\n"
      "s_and_b32 s41, s41, 63\n"
      "s_min_u32 s45, s41, 62\n"
      "s_mul_i32 m0, s45, 1040\n"
      "v_lshl_add_u32 v249, s45, 12, v9\n"
      "s_add_u32 s41, s41, 1\n"
      "buffer_load_dwordx4 v249, s[72:75], 0 offen lds\n"
      "s_and_b32 s41, s41, 63\n"
      "s_waitcnt vmcnt(24)\n"
      "s_barrier\n"
      "v_add_u32 v252, v16, v12\n"
      "v_add_u32 v253, v16, v13\n"
      "v_add_u32 v254, v16, v14\n"
      "v_add_u32 v255, v16, v15\n"
      "ds_read_b32 v244, v252 offset:25600\n"
      "ds_read_b32 v245, v253 offset:25600\n"
      "ds_read_b32 v246, v254 offset:25600\n"
      "ds_read_b32 v247, v255 offset:25600\n"
      "ds_read_b32 v252, v252\n"
      "ds_read_b32 v253, v253\n"
      "ds_read_b32 v254, v254\n"
      "ds_read_b32 v255, v255\n"
      "ds_read_b32 v224, v12\n"
      "ds_read_b32 v225, v13\n"
      "ds_read_b32 v226, v14\n"
      "ds_read_b32 v227, v15\n"
      "ds_read_b32 v228, v12 offset:25600\n"
      "ds_read_b32 v229, v13 offset:25600\n"
      "ds_read_b32 v230, v14 offset:25600\n"
      "ds_read_b32 v231, v15 offset:25600\n"
      "s_min_u32 s45, s41, 62\n"
      "s_mul_i32 m0, s45, 1040\n"
      "v_lshl_add_u32 v248, s45, 12, v9\n"
      "s_add_u32 s41, s41, 1\n"
      "buffer_load_dwordx4 v248, s[72:75], 0 offen lds\n"
      "s_and_b32 s41, s41, 63\n"
      "s_waitcnt vmcnt(23)\n"
      "v_perm_b32 v32, v100, v96, s42\n"
      "v_perm_b32 v36, v100, v96, s43\n"
      "v_perm_b32 v40, v101, v97, s42\n"
      "v_perm_b32 v44, v101, v97, s43\n"
      "s_waitcnt vmcnt(21)\n"
      "v_perm_b32 v33, v108, v104, s42\n"
      "v_perm_b32 v37, v108, v104, s43\n"
      "v_perm_b32 v41, v109, v105, s42\n"
      "v_perm_b32 v45, v109, v105, s43\n"
      "s_min_u32 s45, s41, 62\n"
      "s_mul_i32 m0, s45, 1040\n"
      "v_lshl_add_u32 v249, s45, 12, v9\n"
      "s_add_u32 s41, s41, 1\n"
      "buffer_load_dwordx4 v249, s[72:75], 0 offen lds\n"
      "s_and_b32 s41, s41, 63\n"
      "s_waitcnt vmcnt(20)\n"
      "v_perm_b32 v34, v116, v112, s42\n"
      "v_perm_b32 v38, v116, v112, s43\n"
      "v_perm_b32 v42, v117, v113, s42\n"
      "v_perm_b32 v46, v117, v113, s43\n"
      "s_waitcnt vmcnt(18)\n"
      "v_perm_b32 v35, v124, v120, s42\n"
      "v_perm_b32 v39, v124, v120, s43\n"
      "v_perm_b32 v43, v125, v121, s42\n"
      "v_perm_b32 v47, v125, v121, s43\n"
      "s_min_u32 s45, s41, 62\n"
      "s_mul_i32 m0, s45, 1040\n"
      "v_lshl_add_u32 v248, s45, 12, v9\n"
      "s_add_u32 s41, s41, 1\n"
      "buffer_load_dwordx4 v248, s[72:75], 0 offen lds\n"
      "s_and_b32 s41, s41, 63\n"
      "s_waitcnt lgkmcnt(15)\n"
      "v_mad_u32_u24 v244, v244, s46, v17\n"
      "s_waitcnt lgkmcnt(14)\n"
      "v_mad_u32_u24 v245, v245, s46, v17\n"
      "s_waitcnt lgkmcnt(13)\n"
      "v_mad_u32_u24 v246, v246, s46, v17\n"
      "s_waitcnt lgkmcnt(12)\n"
      "v_mad_u32_u24 v247, v247, s46, v17\n"
      "s_waitcnt lgkmcnt(11)\n"
      "v_mad_u32_u24 v252, v252, s46, v17\n"
      "s_waitcnt lgkmcnt(10)\n"
      "v_mad_u32_u24 v253, v253, s46, v17\n"
      "s_waitcnt lgkmcnt(9)\n"
      "v_mad_u32_u24 v254, v254, s46, v17\n"
      "s_waitcnt lgkmcnt(8)\n"
      "v_mad_u32_u24 v255, v255, s46, v17\n"
      "s_min_u32 s45, s41, 62\n"
      "s_mul_i32 m0, s45, 1040\n"
      "v_lshl_add_u32 v249, s45, 12, v9\n"
      "s_add_u32 s41, s41, 1\n"
      "buffer_load_dwordx4 v249, s[72:75], 0 offen lds\n"
      "s_and_b32 s41, s41, 63\n"
      "v_and_b32 v244, -16, v244\n"
      "v_and_b32 v245, -16, v245\n"
      "v_and_b32 v246, -16, v246\n"
      "v_and_b32 v247, -16, v247\n"
      "v_and_b32 v252, -16, v252\n"
      "v_and_b32 v253, -16, v253\n"
      "v_and_b32 v254, -16, v254\n"
      "v_and_b32 v255, -16, v255\n"
      "s_min_u32 s45, s41, 62\n"
      "s_mul_i32 m0, s45, 1040\n"
      "v_lshl_add_u32 v248, s45, 12, v9\n"
      "s_add_u32 s41, s41, 1\n"
      "buffer_load_dwordx4 v248, s[72:75], 0 offen lds\n"
      "s_and_b32 s41, s41, 63\n"
      "s_waitcnt lgkmcnt(7)\n"
      "v_mul_u32_u24 v224, s46, v224\n"
      "s_waitcnt lgkmcnt(6)\n"
      "v_mul_u32_u24 v225, s46, v225\n"
      "s_waitcnt lgkmcnt(5)\n"
      "v_mul_u32_u24 v226, s46, v226\n"
      "s_waitcnt lgkmcnt(4)\n"
      "v_mul_u32_u24 v227, s46, v227\n"
      "s_waitcnt lgkmcnt(3)\n"
      "v_mul_u32_u24 v228, s46, v228\n"
      "s_waitcnt lgkmcnt(2)\n"
      "v_mul_u32_u24 v229, s46, v229\n"
      "s_waitcnt lgkmcnt(1)\n"
      "v_mul_u32_u24 v230, s46, v230\n"
      "s_waitcnt lgkmcnt(0)\n"
      "v_mul_u32_u24 v231, s46, v231\n"
      "s_min_u32 s45, s41, 62\n"
      "s_mul_i32 m0, s45, 1040\n"
      "v_lshl_add_u32 v249, s45, 12, v9\n"
      "s_add_u32 s41, s41, 1\n"
      "buffer_load_dwordx4 v249, s[72:75], 0 offen lds\n"
      "s_and_b32 s41, s41, 63\n"
      "v_and_b32 v224, -16, v224\n"
      "v_and_b32 v225, -16, v225\n"
      "v_and_b32 v226, -16, v226\n"
      "v_and_b32 v227, -16, v227\n"
      "v_and_b32 v228, -16, v228\n"
      "v_and_b32 v229, -16, v229\n"
      "v_and_b32 v230, -16, v230\n"
      "v_and_b32 v231, -16, v231\n"
      "s_min_u32 s45, s41, 62\n"
      "s_mul_i32 m0, s45, 1040\n"
      "v_lshl_add_u32 v248, s45, 12, v9\n"
      "s_add_u32 s41, s41, 1\n"
      "buffer_load_dwordx4 v248, s[72:75], 0 offen lds\n"
      "s_and_b32 s41, s41, 63\n"
      "s_waitcnt vmcnt(21)\n"
      "v_perm_b32 v240, v132, v128, s42\n"
      "v_perm_b32 v80, v134, v130, s42\n"
      "v_perm_b32 v241, v132, v128, s43\n"
      "v_perm_b32 v84, v134, v130, s43\n"
      "v_cndmask_b32_dpp v64, v80, v240, vcc row_ror:8 row_mask:0xf bank_mask:0xf\n"
      "v_mov_b32_dpp v80, v240 row_ror:8 row_mask:0xf bank_mask:0x3\n"
      "v_perm_b32 v242, v133, v129, s42\n"
      "v_perm_b32 v88, v135, v131, s42\n"
      "s_min_u32 s45, s41, 62\n"
      "s_mul_i32 m0, s45, 1040\n"
      "v_lshl_add_u32 v249, s45, 12, v9\n"
      "s_add_u32 s41, s41, 1\n"
      "buffer_load_dwordx4 v249, s[72:75], 0 offen lds\n"
      "s_and_b32 s41, s41, 63\n"
      "v_cndmask_b32_dpp v68, v84, v241, vcc row_ror:8 row_mask:0xf bank_mask:0xf\n"
      "v_mov_b32_dpp v84, v241 row_ror:8 row_mask:0xf bank_mask:0x3\n"
      "v_perm_b32 v243, v133, v129, s43\n"
      "v_perm_b32 v92, v135, v131, s43\n"
      "v_cndmask_b32_dpp v72, v88, v242, vcc row_ror:8 row_mask:0xf bank_mask:0xf\n"
      "v_mov_b32_dpp v88, v242 row_ror:8 row_mask:0xf bank_mask:0x3\n"
      "s_waitcnt vmcnt(20)\n"
      "v_perm_b32 v240, v140, v136, s42\n"
      "v_perm_b32 v81, v142, v138, s42\n"
      "s_mov_b32 s40, 16384\n"
      "buffer_load_dwordx4 v[160:163], v10, s[68:71], s40 offen\n"
      "v_cndmask_b32_dpp v76, v92, v243, vcc row_ror:8 row_mask:0xf bank_mask:0xf\n"
      "v_mov_b32_dpp v92, v243 row_ror:8 row_mask:0xf bank_mask:0x3\n"
      "v_perm_b32 v241, v140, v136, s43\n"
      "v_perm_b32 v85, v142, v138, s43\n"
      "v_cndmask_b32_dpp v65, v81, v240, vcc row_ror:8 row_mask:0xf bank_mask:0xf\n"
      "v_mov_b32_dpp v81, v240 row_ror:8 row_mask:0xf bank_mask:0x3\n"
      "v_perm_b32 v242, v141, v137, s42\n"
      "v_perm_b32 v89, v143, v139, s42\n"
      "buffer_load_dwordx4 v[164:167], v10, s[68:71], s40 offen offset:1024\n"
      "v_cndmask_b32_dpp v69, v85, v241, vcc row_ror:8 row_mask:0xf bank_mask:0xf\n"
      "v_mov_b32_dpp v85, v241 row_ror:8 row_mask:0xf bank_mask:0x3\n"
      "v_perm_b32 v243, v141, v137, s43\n"
      "v_perm_b32 v93, v143, v139, s43\n"
      "v_cndmask_b32_dpp v73, v89, v242, vcc row_ror:8 row_mask:0xf bank_mask:0xf\n"
      "v_mov_b32_dpp v89, v242 row_ror:8 row_mask:0xf bank_mask:0x3\n"
      "s_waitcnt vmcnt(20)\n"
      "v_perm_b32 v240, v148, v144, s42\n"
      "v_perm_b32 v82, v150, v146, s42\n"
      "buffer_load_dwordx4 v[168:171], v10, s[68:71], s40 offen offset:2048\n"
      "v_cndmask_b32_dpp v77, v93, v243, vcc row_ror:8 row_mask:0xf bank_mask:0xf\n"
      "v_mov_b32_dpp v93, v243 row_ror:8 row_mask:0xf bank_mask:0x3\n"
      "v_perm_b32 v241, v148, v144, s43\n"
      "v_perm_b32 v86, v150, v146, s43\n"
      "v_cndmask_b32_dpp v66, v82, v240, vcc row_ror:8 row_mask:0xf bank_mask:0xf\n"
      "v_mov_b32_dpp v82, v240 row_ror:8 row_mask:0xf bank_mask:0x3\n"
      "v_perm_b32 v242, v149, v145, s42\n"
      "v_perm_b32 v90, v151, v147, s42\n"
      "buffer_load_dwordx4 v[172:175], v10, s[68:71], s40 offen offset:3072\n"
      "v_cndmask_b32_dpp v70, v86, v241, vcc row_ror:8 row_mask:0xf bank_mask:0xf\n"
      "v_mov_b32_dpp v86, v241 row_ror:8 row_mask:0xf bank_mask:0x3\n"
      "v_perm_b32 v243, v149, v145, s43\n"
      "v_perm_b32 v94, v151, v147, s43\n"
      "v_cndmask_b32_dpp v74, v90, v242, vcc row_ror:8 row_mask:0xf bank_mask:0xf\n"
      "v_mov_b32_dpp v90, v242 row_ror:8 row_mask:0xf bank_mask:0x3\n"
      "s_waitcnt vmcnt(20)\n"
      "v_perm_b32 v240, v156, v152, s42\n"
      "v_perm_b32 v83, v158, v154, s42\n"
      "s_mov_b32 s40, 20480\n"
      "buffer_load_dwordx4 v[176:179], v10, s[68:71], s40 offen\n"
      "v_cndmask_b32_dpp v78, v94, v243, vcc row_ror:8 row_mask:0xf bank_mask:0xf\n"
      "v_mov_b32_dpp v94, v243 row_ror:8 row_mask:0xf bank_mask:0x3\n"
      "v_perm_b32 v241, v156, v152, s43\n"
      "v_perm_b32 v87, v158, v154, s43\n"
      "v_cndmask_b32_dpp v67, v83, v240, vcc row_ror:8 row_mask:0xf bank_mask:0xf\n"
      "v_mov_b32_dpp v83, v240 row_ror:8 row_mask:0xf bank_mask:0x3\n"
      "v_perm_b32 v242, v157, v153, s42\n"
      "v_perm_b32 v91, v159, v155, s42\n"
      "buffer_load_dwordx4 v[180:183], v10, s[68:71], s40 offen offset:1024\n"
      "v_cndmask_b32_dpp v71, v87, v241, vcc row_ror:8 row_mask:0xf bank_mask:0xf\n"
      "v_mov_b32_dpp v87, v241 row_ror:8 row_mask:0xf bank_mask:0x3\n"
      "v_perm_b32 v243, v157, v153, s43\n"
      "v_perm_b32 v95, v159, v155, s43\n"
      "v_cndmask_b32_dpp v75, v91, v242, vcc row_ror:8 row_mask:0xf bank_mask:0xf\n"
      "v_mov_b32_dpp v91, v242 row_ror:8 row_mask:0xf bank_mask:0x3\n"
      "v_cndmask_b32_dpp v79, v95, v243, vcc row_ror:8 row_mask:0xf bank_mask:0xf\n"
      "v_mov_b32_dpp v95, v243 row_ror:8 row_mask:0xf bank_mask:0x3\n"
      "buffer_load_dwordx4 v[184:187], v10, s[68:71], s40 offen offset:2048\n"
      "buffer_load_dwordx4 v[188:191], v10, s[68:71], s40 offen offset:3072\n"
      "s_mov_b32 exec_lo, 0x0\n"
      "s_mov_b32 exec_hi, 0xffffffff\n"
      "buffer_load_dword a236, v31, s[68:71], 0 offen\n"
      "s_mov_b64 exec, -1\n"
      "s_waitcnt vmcnt(9)\n"
      "s_barrier\n"
      "ds_read_b128 a[128:131], v252\n"
      "ds_read_b128 a[132:135], v253\n"
      "ds_read_b128 a[136:139], v254\n"
      "ds_read_b128 a[140:143], v255\n"
      "ds_read_b128 a[144:147], v244\n"
      "ds_read_b128 a[148:151], v245\n"
      "ds_read_b128 a[152:155], v246\n"
      "s_waitcnt lgkmcnt(6)\n"
      "v_mfma_f32_16x16x32_bf16 a[0:3], a[128:131], v[32:35], 0\n"
      "ds_read_b128 a[156:159], v247\n"
      "s_mov_b32 s40, 2097152\n"
      "v_mfma_f32_16x16x32_bf16 a[4:7], a[128:131], v[36:39], 0\n"
      "buffer_load_dwordx4 v[192:195], v10, s[68:71], s40 offen\n"
      "v_mfma_f32_16x16x32_bf16 a[8:11], a[128:131], v[40:43], 0\n"
      "ds_read_b32 v232, v12 offset:64\n"
      "v_mfma_f32_16x16x32_bf16 a[12:15], a[128:131], v[44:47], 0\n"
      "s_waitcnt lgkmcnt(7)\n"
      "v_mfma_f32_16x16x32_bf16 a[16:19], a[132:135], v[32:35], 0\n"
      "ds_read_b128 a[160:163], v224\n"
      "v_mfma_f32_16x16x32_bf16 a[20:23], a[132:135], v[36:39], 0\n"
      "ds_read_b32 v233, v13 offset:64\n"
      "v_mfma_f32_16x16x32_bf16 a[24:27], a[132:135], v[40:43], 0\n"
      "buffer_load_dwordx4 v[196:199], v10, s[68:71], s40 offen offset:1024\n"
      "v_mfma_f32_16x16x32_bf16 a[28:31], a[132:135], v[44:47], 0\n"
      "s_waitcnt lgkmcnt(8)\n"
      "v_mfma_f32_16x16x32_bf16 a[32:35], a[136:139], v[32:35], 0\n"
      "ds_read_b128 a[164:167], v225\n"
      "v_mfma_f32_16x16x32_bf16 a[36:39], a[136:139], v[36:39], 0\n"
      "ds_read_b32 v234, v14 offset:64\n"
      "v_mfma_f32_16x16x32_bf16 a[40:43], a[136:139], v[40:43], 0\n"
      "ds_read_b32 v235, v15 offset:64\n"
      "v_mfma_f32_16x16x32_bf16 a[44:47], a[136:139], v[44:47], 0\n"
      "s_waitcnt lgkmcnt(10)\n"
      "v_mfma_f32_16x16x32_bf16 a[48:51], a[140:143], v[32:35], 0\n"
      "ds_read_b128 a[168:171], v226\n"
      "v_mfma_f32_16x16x32_bf16 a[52:55], a[140:143], v[36:39], 0\n"
      "buffer_load_dwordx4 v[200:203], v10, s[68:71], s40 offen offset:2048\n"
      "v_mfma_f32_16x16x32_bf16 a[56:59], a[140:143], v[40:43], 0\n"
      "ds_read_b32 v236, v12 offset:25664\n"
      "v_mfma_f32_16x16x32_bf16 a[60:63], a[140:143], v[44:47], 0\n"
      "s_waitcnt lgkmcnt(11)\n"
      "v_mfma_f32_16x16x32_bf16 a[64:67], a[144:147], v[32:35], 0\n"
      "ds_read_b128 a[172:175], v227\n"
      "v_mfma_f32_16x16x32_bf16 a[68:71], a[144:147], v[36:39], 0\n"
      "ds_read_b32 v237, v13 offset:25664\n"
      "v_mfma_f32_16x16x32_bf16 a[72:75], a[144:147], v[40:43], 0\n"
      "buffer_load_dwordx4 v[204:207], v10, s[68:71], s40 offen offset:3072\n"
      "v_mfma_f32_16x16x32_bf16 a[76:79], a[144:147], v[44:47], 0\n"
      "s_mov_b32 s40, 2101248\n"
      "s_waitcnt lgkmcnt(12)\n"
      "v_mfma_f32_16x16x32_bf16 a[80:83], a[148:151], v[32:35], 0\n"
      "ds_read_b128 a[176:179], v228\n"
      "v_mfma_f32_16x16x32_bf16 a[84:87], a[148:151], v[36:39], 0\n"
      "ds_read_b32 v238, v14 offset:25664\n"
      "v_mfma_f32_16x16x32_bf16 a[88:91], a[148:151], v[40:43], 0\n"
      "ds_read_b32 v239, v15 offset:25664\n"
      "v_mfma_f32_16x16x32_bf16 a[92:95], a[148:151], v[44:47], 0\n"
      "s_waitcnt lgkmcnt(14)\n"
      "v_mfma_f32_16x16x32_bf16 a[96:99], a[152:155], v[32:35], 0\n"
      "ds_read_b128 a[180:183], v229\n"
      "v_mfma_f32_16x16x32_bf16 a[100:103], a[152:155], v[36:39], 0\n"
      "buffer_load_dwordx4 v[208:211], v10, s[68:71], s40 offen\n"
      "v_mfma_f32_16x16x32_bf16 a[104:107], a[152:155], v[40:43], 0\n"
      "v_mfma_f32_16x16x32_bf16 a[108:111], a[152:155], v[44:47], 0\n"
      "s_waitcnt lgkmcnt(14)\n"
      "v_mfma_f32_16x16x32_bf16 a[112:115], a[156:159], v[32:35], 0\n"
      "ds_read_b128 a[128:131], v230\n"
      "v_mfma_f32_16x16x32_bf16 a[116:119], a[156:159], v[36:39], 0\n"
      "v_mfma_f32_16x16x32_bf16 a[120:123], a[156:159], v[40:43], 0\n"
      "buffer_load_dwordx4 v[212:215], v10, s[68:71], s40 offen offset:1024\n"
      "v_mfma_f32_16x16x32_bf16 a[124:127], a[156:159], v[44:47], 0\n"
      "s_waitcnt lgkmcnt(6)\n"
      "v_mfma_f32_16x16x32_bf16 a[0:3], a[160:163], v[64:67], a[0:3]\n"
      "ds_read_b128 a[132:135], v231\n"
      "s_waitcnt vmcnt(13)\n"
      "v_perm_b32 v240, v164, v160, s42\n"
      "v_mfma_f32_16x16x32_bf16 a[4:7], a[160:163], v[68:71], a[4:7]\n"
      "v_perm_b32 v48, v166, v162, s42\n"
      "v_perm_b32 v241, v164, v160, s43\n"
      "v_mfma_f32_16x16x32_bf16 a[8:11], a[160:163], v[72:75], a[8:11]\n"
      "v_perm_b32 v52, v166, v162, s43\n"
      "v_cndmask_b32_dpp v32, v48, v240, vcc row_ror:8 row_mask:0xf bank_mask:0xf\n"
      "v_mfma_f32_16x16x32_bf16 a[12:15], a[160:163], v[76:79], a[12:15]\n"
      "buffer_load_dwordx4 v[216:219], v10, s[68:71], s40 offen offset:2048\n"
      "v_mfma_f32_16x16x32_bf16 a[16:19], a[164:167], v[64:67], a[16:19]\n"
      "ds_read_b128 a[136:139], v224 offset:16\n"
      "v_mov_b32_dpp v48, v240 row_ror:8 row_mask:0xf bank_mask:0x3\n"
      "v_perm_b32 v242, v165, v161, s42\n"
      "v_mfma_f32_16x16x32_bf16 a[20:23], a[164:167], v[68:71], a[20:23]\n"
      "v_perm_b32 v56, v167, v163, s42\n"
      "v_cndmask_b32_dpp v36, v52, v241, vcc row_ror:8 row_mask:0xf bank_mask:0xf\n"
      "v_mfma_f32_16x16x32_bf16 a[24:27], a[164:167], v[72:75], a[24:27]\n"
      "v_mov_b32_dpp v52, v241 row_ror:8 row_mask:0xf bank_mask:0x3\n"
      "v_mfma_f32_16x16x32_bf16 a[28:31], a[164:167], v[76:79], a[28:31]\n"
      "v_perm_b32 v243, v165, v161, s43\n"
      "v_mfma_f32_16x16x32_bf16 a[32:35], a[168:171], v[64:67], a[32:35]\n"
      "ds_read_b128 a[140:143], v225 offset:16\n"
      "v_perm_b32 v60, v167, v163, s43\n"
      "v_mfma_f32_16x16x32_bf16 a[36:39], a[168:171], v[68:71], a[36:39]\n"
      "buffer_load_dwordx4 v[220:223], v10, s[68:71], s40 offen offset:3072\n"
      "v_mfma_f32_16x16x32_bf16 a[40:43], a[168:171], v[72:75], a[40:43]\n"
      "s_mov_b32 s40, 2113536\n"
      "v_mul_u32_u24 v232, s46, v232\n"
      "v_cndmask_b32_dpp v40, v56, v242, vcc row_ror:8 row_mask:0xf bank_mask:0xf\n"
      "v_mfma_f32_16x16x32_bf16 a[44:47], a[168:171], v[76:79], a[44:47]\n"
      "v_mov_b32_dpp v56, v242 row_ror:8 row_mask:0xf bank_mask:0x3\n"
      "s_waitcnt vmcnt(13)\n"
      "v_perm_b32 v240, v172, v168, s42\n"
      "v_mfma_f32_16x16x32_bf16 a[48:51], a[172:175], v[64:67], a[48:51]\n"
      "ds_read_b128 a[144:147], v226 offset:16\n"
      "v_perm_b32 v49, v174, v170, s42\n"
      "v_cndmask_b32_dpp v44, v60, v243, vcc row_ror:8 row_mask:0xf bank_mask:0xf\n"
      "v_mfma_f32_16x16x32_bf16 a[52:55], a[172:175], v[68:71], a[52:55]\n"
      "v_mul_u32_u24 v233, s46, v233\n"
      "v_mov_b32_dpp v60, v243 row_ror:8 row_mask:0xf bank_mask:0x3\n"
      "v_mfma_f32_16x16x32_bf16 a[56:59], a[172:175], v[72:75], a[56:59]\n"
      "buffer_load_dwordx4 v[96:99], v10, s[68:71], s40 offen\n"
      "v_mfma_f32_16x16x32_bf16 a[60:63], a[172:175], v[76:79], a[60:63]\n"
      "v_perm_b32 v241, v172, v168, s43\n"
      "v_perm_b32 v53, v174, v170, s43\n"
      "s_waitcnt lgkmcnt(3)\n"
      "v_mfma_f32_16x16x32_bf16 a[64:67], a[176:179], v[64:67], a[64:67]\n"
      "ds_read_b128 a[148:151], v227 offset:16\n"
      "v_cndmask_b32_dpp v33, v49, v240, vcc row_ror:8 row_mask:0xf bank_mask:0xf\n"
      "v_mov_b32_dpp v49, v240 row_ror:8 row_mask:0xf bank_mask:0x3\n"
      "v_mfma_f32_16x16x32_bf16 a[68:71], a[176:179], v[68:71], a[68:71]\n"
      "v_mul_u32_u24 v234, s46, v234\n"
      "v_perm_b32 v242, v173, v169, s42\n"
      "v_mfma_f32_16x16x32_bf16 a[72:75], a[176:179], v[72:75], a[72:75]\n"
      "v_mul_u32_u24 v235, s46, v235\n"
      "v_perm_b32 v57, v175, v171, s42\n"
      "v_mfma_f32_16x16x32_bf16 a[76:79], a[176:179], v[76:79], a[76:79]\n"
      "buffer_load_dwordx4 v[100:103], v10, s[68:71], s40 offen offset:1024\n"
      "v_mfma_f32_16x16x32_bf16 a[80:83], a[180:183], v[64:67], a[80:83]\n"
      "ds_read_b128 a[152:155], v228 offset:16\n"
      "v_cndmask_b32_dpp v37, v53, v241, vcc row_ror:8 row_mask:0xf bank_mask:0xf\n"
      "v_mov_b32_dpp v53, v241 row_ror:8 row_mask:0xf bank_mask:0x3\n"
      "v_mfma_f32_16x16x32_bf16 a[84:87], a[180:183], v[68:71], a[84:87]\n"
      "v_mul_u32_u24 v236, s46, v236\n"
      "v_perm_b32 v243, v173, v169, s43\n"
      "v_mfma_f32_16x16x32_bf16 a[88:91], a[180:183], v[72:75], a[88:91]\n"
      "v_mul_u32_u24 v237, s46, v237\n"
      "v_perm_b32 v61, v175, v171, s43\n"
      "v_mfma_f32_16x16x32_bf16 a[92:95], a[180:183], v[76:79], a[92:95]\n"
      "v_cndmask_b32_dpp v41, v57, v242, vcc row_ror:8 row_mask:0xf bank_mask:0xf\n"
      "v_mov_b32_dpp v57, v242 row_ror:8 row_mask:0xf bank_mask:0x3\n"
      "v_mfma_f32_16x16x32_bf16 a[96:99], a[128:131], v[64:67], a[96:99]\n"
      "ds_read_b128 a[156:159], v229 offset:16\n"
      "s_waitcnt vmcnt(13)\n"
      "v_perm_b32 v240, v180, v176, s42\n"
      "v_mfma_f32_16x16x32_bf16 a[100:103], a[128:131], v[68:71], a[100:103]\n"
      "buffer_load_dwordx4 v[104:107], v10, s[68:71], s40 offen offset:2048\n"
      "v_mfma_f32_16x16x32_bf16 a[104:107], a[128:131], v[72:75], a[104:107]\n"
      "v_mul_u32_u24 v238, s46, v238\n"
      "v_perm_b32 v50, v182, v178, s42\n"
      "v_mfma_f32_16x16x32_bf16 a[108:111], a[128:131], v[76:79], a[108:111]\n"
      "v_cndmask_b32_dpp v45, v61, v243, vcc row_ror:8 row_mask:0xf bank_mask:0xf\n"
      "v_mov_b32_dpp v61, v243 row_ror:8 row_mask:0xf bank_mask:0x3\n"
      "v_mfma_f32_16x16x32_bf16 a[112:115], a[132:135], v[64:67], a[112:115]\n"
      "ds_read_b128 a[160:163], v230 offset:16\n"
      "v_perm_b32 v241, v180, v176, s43\n"
      "v_perm_b32 v54, v182, v178, s43\n"
      "v_mfma_f32_16x16x32_bf16 a[116:119], a[132:135], v[68:71], a[116:119]\n"
      "v_mul_u32_u24 v239, s46, v239\n"
      "v_cndmask_b32_dpp v34, v50, v240, vcc row_ror:8 row_mask:0xf bank_mask:0xf\n"
      "v_mfma_f32_16x16x32_bf16 a[120:123], a[132:135], v[72:75], a[120:123]\n"
      "buffer_load_dwordx4 v[108:111], v10, s[68:71], s40 offen offset:3072\n"
      "v_mfma_f32_16x16x32_bf16 a[124:127], a[132:135], v[76:79], a[124:127]\n"
      "s_mov_b32 s40, 2117632\n"
      "v_mov_b32_dpp v50, v240 row_ror:8 row_mask:0xf bank_mask:0x3\n"
      "s_waitcnt lgkmcnt(3)\n"
      "v_mfma_f32_16x16x32_bf16 a[0:3], a[136:139], v[80:83], a[0:3]\n"
      "ds_read_b128 a[164:167], v231 offset:16\n"
      "v_perm_b32 v242, v181, v177, s42\n"
      "v_perm_b32 v58, v183, v179, s42\n"
      "v_mfma_f32_16x16x32_bf16 a[4:7], a[136:139], v[84:87], a[4:7]\n"
      "v_and_b32 v232, -16, v232\n"
      "v_cndmask_b32_dpp v38, v54, v241, vcc row_ror:8 row_mask:0xf bank_mask:0xf\n"
      "v_mfma_f32_16x16x32_bf16 a[8:11], a[136:139], v[88:91], a[8:11]\n"
      "v_and_b32 v233, -16, v233\n"
      "v_mov_b32_dpp v54, v241 row_ror:8 row_mask:0xf bank_mask:0x3\n"
      "v_mfma_f32_16x16x32_bf16 a[12:15], a[136:139], v[92:95], a[12:15]\n"
      "buffer_load_dwordx4 v[112:115], v10, s[68:71], s40 offen\n"
      "v_mfma_f32_16x16x32_bf16 a[16:19], a[140:143], v[80:83], a[16:19]\n"
      "ds_read_b128 a[168:171], v224 offset:32\n"
      "v_perm_b32 v243, v181, v177, s43\n"
      "v_perm_b32 v62, v183, v179, s43\n"
      "v_mfma_f32_16x16x32_bf16 a[20:23], a[140:143], v[84:87], a[20:23]\n"
      "v_and_b32 v234, -16, v234\n"
      "v_cndmask_b32_dpp v42, v58, v242, vcc row_ror:8 row_mask:0xf bank_mask:0xf\n"
      "v_mfma_f32_16x16x32_bf16 a[24:27], a[140:143], v[88:91], a[24:27]\n"
      "v_and_b32 v235, -16, v235\n"
      "v_mov_b32_dpp v58, v242 row_ror:8 row_mask:0xf bank_mask:0x3\n"
      "v_mfma_f32_16x16x32_bf16 a[28:31], a[140:143], v[92:95], a[28:31]\n"
      "s_waitcnt vmcnt(14)\n"
      "v_perm_b32 v240, v188, v184, s42\n"
      "v_perm_b32 v51, v190, v186, s42\n"
      "v_mfma_f32_16x16x32_bf16 a[32:35], a[144:147], v[80:83], a[32:35]\n"
      "ds_read_b128 a[172:175], v225 offset:32\n"
      "v_cndmask_b32_dpp v46, v62, v243, vcc row_ror:8 row_mask:0xf bank_mask:0xf\n"
      "v_mov_b32_dpp v62, v243 row_ror:8 row_mask:0xf bank_mask:0x3\n"
      "v_mfma_f32_16x16x32_bf16 a[36:39], a[144:147], v[84:87], a[36:39]\n"
      "buffer_load_dwordx4 v[116:119], v10, s[68:71], s40 offen offset:1024\n"
      "v_mfma_f32_16x16x32_bf16 a[40:43], a[144:147], v[88:91], a[40:43]\n"
      "v_and_b32 v236, -16, v236\n"
      "v_perm_b32 v241, v188, v184, s43\n"
      "v_mfma_f32_16x16x32_bf16 a[44:47], a[144:147], v[92:95], a[44:47]\n"
      "v_perm_b32 v55, v190, v186, s43\n"
      "v_cndmask_b32_dpp v35, v51, v240, vcc row_ror:8 row_mask:0xf bank_mask:0xf\n"
      "v_mfma_f32_16x16x32_bf16 a[48:51], a[148:151], v[80:83], a[48:51]\n"
      "ds_read_b128 a[176:179], v226 offset:32\n"
      "v_mov_b32_dpp v51, v240 row_ror:8 row_mask:0xf bank_mask:0x3\n"
      "v_perm_b32 v242, v189, v185, s42\n"
      "v_mfma_f32_16x16x32_bf16 a[52:55], a[148:151], v[84:87], a[52:55]\n"
      "v_and_b32 v237, -16, v237\n"
      "v_perm_b32 v59, v191, v187, s42\n"
      "v_mfma_f32_16x16x32_bf16 a[56:59], a[148:151], v[88:91], a[56:59]\n"
      "buffer_load_dwordx4 v[120:123], v10, s[68:71], s40 offen offset:2048\n"
      "v_mfma_f32_16x16x32_bf16 a[60:63], a[148:151], v[92:95], a[60:63]\n"
      "v_cndmask_b32_dpp v39, v55, v241, vcc row_ror:8 row_mask:0xf bank_mask:0xf\n"
      "v_mov_b32_dpp v55, v241 row_ror:8 row_mask:0xf bank_mask:0x3\n"
      "s_waitcnt lgkmcnt(3)\n"
      "v_mfma_f32_16x16x32_bf16 a[64:67], a[152:155], v[80:83], a[64:67]\n"
      "ds_read_b128 a[180:183], v227 offset:32\n"
      "v_perm_b32 v243, v189, v185, s43\n"
      "v_perm_b32 v63, v191, v187, s43\n"
      "v_mfma_f32_16x16x32_bf16 a[68:71], a[152:155], v[84:87], a[68:71]\n"
      "v_and_b32 v238, -16, v238\n"
      "v_cndmask_b32_dpp v43, v59, v242, vcc row_ror:8 row_mask:0xf bank_mask:0xf\n"
      "v_mfma_f32_16x16x32_bf16 a[72:75], a[152:155], v[88:91], a[72:75]\n"
      "ds_read_b128 a[128:131], v228 offset:32\n"
      "v_and_b32 v239, -16, v239\n"
      "v_mov_b32_dpp v59, v242 row_ror:8 row_mask:0xf bank_mask:0x3\n"
      "v_mfma_f32_16x16x32_bf16 a[76:79], a[152:155], v[92:95], a[76:79]\n"
      "buffer_load_dwordx4 v[124:127], v10, s[68:71], s40 offen offset:3072\n"
      "v_mfma_f32_16x16x32_bf16 a[80:83], a[156:159], v[80:83], a[80:83]\n"
      "ds_read_b128 a[132:135], v229 offset:32\n"
      "s_mov_b32 s40, 4194304\n"
      "v_cndmask_b32_dpp v47, v63, v243, vcc row_ror:8 row_mask:0xf bank_mask:0xf\n"
      "v_mfma_f32_16x16x32_bf16 a[84:87], a[156:159], v[84:87], a[84:87]\n"
      "v_mov_b32_dpp v63, v243 row_ror:8 row_mask:0xf bank_mask:0x3\n"
      "v_mfma_f32_16x16x32_bf16 a[88:91], a[156:159], v[88:91], a[88:91]\n"
      "v_mfma_f32_16x16x32_bf16 a[92:95], a[156:159], v[92:95], a[92:95]\n"
      "v_mfma_f32_16x16x32_bf16 a[96:99], a[160:163], v[80:83], a[96:99]\n"
      "ds_read_b128 a[136:139], v230 offset:32\n"
      "v_mfma_f32_16x16x32_bf16 a[100:103], a[160:163], v[84:87], a[100:103]\n"
      "buffer_load_dwordx4 v[128:131], v10, s[68:71], s40 offen\n"
      "v_mfma_f32_16x16x32_bf16 a[104:107], a[160:163], v[88:91], a[104:107]\n"
      "v_mfma_f32_16x16x32_bf16 a[108:111], a[160:163], v[92:95], a[108:111]\n"
      "v_mfma_f32_16x16x32_bf16 a[112:115], a[164:167], v[80:83], a[112:115]\n"
      "ds_read_b128 a[140:143], v231 offset:32\n"
      "v_mfma_f32_16x16x32_bf16 a[116:119], a[164:167], v[84:87], a[116:119]\n"
      "v_mfma_f32_16x16x32_bf16 a[120:123], a[164:167], v[88:91], a[120:123]\n"
      "buffer_load_dwordx4 v[132:135], v10, s[68:71], s40 offen offset:1024\n"
      "v_mfma_f32_16x16x32_bf16 a[124:127], a[164:167], v[92:95], a[124:127]\n"
      "s_waitcnt lgkmcnt(4)\n"
      "v_mfma_f32_16x16x32_bf16 a[0:3], a[168:171], v[32:35], a[0:3]\n"
      "ds_read_b128 a[144:147], v224 offset:48\n"
      "s_waitcnt vmcnt(16)\n"
      "v_perm_b32 v240, v196, v192, s42\n"
      "v_mfma_f32_16x16x32_bf16 a[4:7], a[168:171], v[36:39], a[4:7]\n"
      "v_perm_b32 v80, v198, v194, s42\n"
      "v_perm_b32 v241, v196, v192, s43\n"
      "v_mfma_f32_16x16x32_bf16 a[8:11], a[168:171], v[40:43], a[8:11]\n"
      "ds_read_b128 a[148:151], v225 offset:48\n"
      "v_perm_b32 v84, v198, v194, s43\n"
      "v_cndmask_b32_dpp v64, v80, v240, vcc row_ror:8 row_mask:0xf bank_mask:0xf\n"
      "v_mfma_f32_16x16x32_bf16 a[12:15], a[168:171], v[44:47], a[12:15]\n"
      "buffer_load_dwordx4 v[136:139], v10, s[68:71], s40 offen offset:2048\n"
      "v_mfma_f32_16x16x32_bf16 a[16:19], a[172:175], v[32:35], a[16:19]\n"
      "ds_read_b128 a[152:155], v226 offset:48\n"
      "v_mov_b32_dpp v80, v240 row_ror:8 row_mask:0xf bank_mask:0x3\n"
      "v_perm_b32 v242, v197, v193, s42\n"
      "v_mfma_f32_16x16x32_bf16 a[20:23], a[172:175], v[36:39], a[20:23]\n"
      "v_add_u32 v31, 8388608, v31\n"
      "s_mov_b32 exec_lo, 0xffffffff\n"
      "s_mov_b32 exec_hi, 0x0\n"
      "buffer_load_dword a236, v31, s[68:71], 0 offen\n"
      "s_mov_b64 exec, -1\n"
      "v_mfma_f32_16x16x32_bf16 a[24:27], a[172:175], v[40:43], a[24:27]\n"
      "v_perm_b32 v88, v199, v195, s42\n"
      "v_cndmask_b32_dpp v68, v84, v241, vcc row_ror:8 row_mask:0xf bank_mask:0xf\n"
      "v_mfma_f32_16x16x32_bf16 a[28:31], a[172:175], v[44:47], a[28:31]\n"
      "v_mov_b32_dpp v84, v241 row_ror:8 row_mask:0xf bank_mask:0x3\n"
      "v_perm_b32 v243, v197, v193, s43\n"
      "v_mfma_f32_16x16x32_bf16 a[32:35], a[176:179], v[32:35], a[32:35]\n"
      "ds_read_b128 a[156:159], v227 offset:48\n"
      "v_perm_b32 v92, v199, v195, s43\n"
      "v_mfma_f32_16x16x32_bf16 a[36:39], a[176:179], v[36:39], a[36:39]\n"
      "buffer_load_dwordx4 v[140:143], v10, s[68:71], s40 offen offset:3072\n"
      "v_mfma_f32_16x16x32_bf16 a[40:43], a[176:179], v[40:43], a[40:43]\n"
      "s_mov_b32 s40, 4198400\n"
      "v_cndmask_b32_dpp v72, v88, v242, vcc row_ror:8 row_mask:0xf bank_mask:0xf\n"
      "v_mov_b32_dpp v88, v242 row_ror:8 row_mask:0xf bank_mask:0x3\n"
      "v_mfma_f32_16x16x32_bf16 a[44:47], a[176:179], v[44:47], a[44:47]\n"
      "s_waitcnt vmcnt(17)\n"
      "v_perm_b32 v240, v204, v200, s42\n"
      "v_perm_b32 v81, v206, v202, s42\n"
      "v_mfma_f32_16x16x32_bf16 a[48:51], a[180:183], v[32:35], a[48:51]\n"
      "ds_read_b128 a[160:163], v228 offset:48\n"
      "v_cndmask_b32_dpp v76, v92, v243, vcc row_ror:8 row_mask:0xf bank_mask:0xf\n"
      "v_mfma_f32_16x16x32_bf16 a[52:55], a[180:183], v[36:39], a[52:55]\n"
      "v_mov_b32_dpp v92, v243 row_ror:8 row_mask:0xf bank_mask:0x3\n"
      "v_mfma_f32_16x16x32_bf16 a[56:59], a[180:183], v[40:43], a[56:59]\n"
      "buffer_load_dwordx4 v[144:147], v10, s[68:71], s40 offen\n"
      "v_mfma_f32_16x16x32_bf16 a[60:63], a[180:183], v[44:47], a[60:63]\n"
      "v_perm_b32 v241, v204, v200, s43\n"
      "v_perm_b32 v85, v206, v202, s43\n"
      "s_waitcnt lgkmcnt(5)\n"
      "v_mfma_f32_16x16x32_bf16 a[64:67], a[128:131], v[32:35], a[64:67]\n"
      "ds_read_b128 a[164:167], v229 offset:48\n"
      "v_cndmask_b32_dpp v65, v81, v240, vcc row_ror:8 row_mask:0xf bank_mask:0xf\n"
      "v_mov_b32_dpp v81, v240 row_ror:8 row_mask:0xf bank_mask:0x3\n"
      "v_mfma_f32_16x16x32_bf16 a[68:71], a[128:131], v[36:39], a[68:71]\n"
      "v_perm_b32 v242, v205, v201, s42\n"
      "v_mfma_f32_16x16x32_bf16 a[72:75], a[128:131], v[40:43], a[72:75]\n"
      "ds_read_b128 a[168:171], v230 offset:48\n"
      "v_perm_b32 v89, v207, v203, s42\n"
      "v_mfma_f32_16x16x32_bf16 a[76:79], a[128:131], v[44:47], a[76:79]\n"
      "buffer_load_dwordx4 v[148:151], v10, s[68:71], s40 offen offset:1024\n"
      "v_mfma_f32_16x16x32_bf16 a[80:83], a[132:135], v[32:35], a[80:83]\n"
      "ds_read_b128 a[172:175], v231 offset:48\n"
      "v_cndmask_b32_dpp v69, v85, v241, vcc row_ror:8 row_mask:0xf bank_mask:0xf\n"
      "v_mov_b32_dpp v85, v241 row_ror:8 row_mask:0xf bank_mask:0x3\n"
      "v_mfma_f32_16x16x32_bf16 a[84:87], a[132:135], v[36:39], a[84:87]\n"
      "ds_read_b32 v224, v12 offset:128\n"
      "v_perm_b32 v243, v205, v201, s43\n"
      "v_perm_b32 v93, v207, v203, s43\n"
      "v_mfma_f32_16x16x32_bf16 a[88:91], a[132:135], v[40:43], a[88:91]\n"
      "ds_read_b32 v225, v13 offset:128\n"
      "v_cndmask_b32_dpp v73, v89, v242, vcc row_ror:8 row_mask:0xf bank_mask:0xf\n"
      "v_mfma_f32_16x16x32_bf16 a[92:95], a[132:135], v[44:47], a[92:95]\n"
      "v_mov_b32_dpp v89, v242 row_ror:8 row_mask:0xf bank_mask:0x3\n"
      "v_mfma_f32_16x16x32_bf16 a[96:99], a[136:139], v[32:35], a[96:99]\n"
      "ds_read_b128 a[176:179], v232\n"
      "s_waitcnt vmcnt(17)\n"
      "v_perm_b32 v240, v212, v208, s42\n"
      "v_mfma_f32_16x16x32_bf16 a[100:103], a[136:139], v[36:39], a[100:103]\n"
      "buffer_load_dwordx4 v[152:155], v10, s[68:71], s40 offen offset:2048\n"
      "v_mfma_f32_16x16x32_bf16 a[104:107], a[136:139], v[40:43], a[104:107]\n"
      "ds_read_b32 v226, v14 offset:128\n"
      "v_perm_b32 v82, v214, v210, s42\n"
      "v_cndmask_b32_dpp v77, v93, v243, vcc row_ror:8 row_mask:0xf bank_mask:0xf\n"
      "v_mfma_f32_16x16x32_bf16 a[108:111], a[136:139], v[44:47], a[108:111]\n"
      "v_mov_b32_dpp v93, v243 row_ror:8 row_mask:0xf bank_mask:0x3\n"
      "v_perm_b32 v241, v212, v208, s43\n"
      "v_mfma_f32_16x16x32_bf16 a[112:115], a[140:143], v[32:35], a[112:115]\n"
      "ds_read_b128 a[180:183], v233\n"
      "v_perm_b32 v86, v214, v210, s43\n"
      "v_mfma_f32_16x16x32_bf16 a[116:119], a[140:143], v[36:39], a[116:119]\n"
      "ds_read_b32 v227, v15 offset:128\n"
      "v_cndmask_b32_dpp v66, v82, v240, vcc row_ror:8 row_mask:0xf bank_mask:0xf\n"
      "v_mfma_f32_16x16x32_bf16 a[120:123], a[140:143], v[40:43], a[120:123]\n"
      "buffer_load_dwordx4 v[156:159], v10, s[68:71], s40 offen offset:3072\n"
      "v_mfma_f32_16x16x32_bf16 a[124:127], a[140:143], v[44:47], a[124:127]\n"
      "s_mov_b32 s40, 4210688\n"
      "v_mov_b32_dpp v82, v240 row_ror:8 row_mask:0xf bank_mask:0x3\n"
      "s_waitcnt lgkmcnt(10)\n"
      "v_mfma_f32_16x16x32_bf16 a[0:3], a[144:147], v[48:51], a[0:3]\n"
      "ds_read_b128 a[128:131], v234\n"
      "v_perm_b32 v242, v213, v209, s42\n"
      "v_perm_b32 v90, v215, v211, s42\n"
      "v_mfma_f32_16x16x32_bf16 a[4:7], a[144:147], v[52:55], a[4:7]\n"
      "ds_read_b32 v228, v12 offset:25728\n"
      "v_cndmask_b32_dpp v70, v86, v241, vcc row_ror:8 row_mask:0xf bank_mask:0xf\n"
      "v_mov_b32_dpp v86, v241 row_ror:8 row_mask:0xf bank_mask:0x3\n"
      "v_mfma_f32_16x16x32_bf16 a[8:11], a[144:147], v[56:59], a[8:11]\n"
      "ds_read_b128 a[132:135], v235\n"
      "ds_read_b32 v229, v13 offset:25728\n"
      "v_perm_b32 v243, v213, v209, s43\n"
      "v_mfma_f32_16x16x32_bf16 a[12:15], a[144:147], v[60:63], a[12:15]\n"
      "buffer_load_dwordx4 v[160:163], v10, s[68:71], s40 offen\n"
      "v_mfma_f32_16x16x32_bf16 a[16:19], a[148:151], v[48:51], a[16:19]\n"
      "ds_read_b128 a[136:139], v236\n"
      "v_perm_b32 v94, v215, v211, s43\n"
      "v_cndmask_b32_dpp v74, v90, v242, vcc row_ror:8 row_mask:0xf bank_mask:0xf\n"
      "v_mfma_f32_16x16x32_bf16 a[20:23], a[148:151], v[52:55], a[20:23]\n"
      "ds_read_b32 v230, v14 offset:25728\n"
      "v_mov_b32_dpp v90, v242 row_ror:8 row_mask:0xf bank_mask:0x3\n"
      "s_waitcnt vmcnt(18)\n"
      "v_perm_b32 v240, v220, v216, s42\n"
      "v_mfma_f32_16x16x32_bf16 a[24:27], a[148:151], v[56:59], a[24:27]\n"
      "ds_read_b32 v231, v15 offset:25728\n"
      "v_perm_b32 v83, v222, v218, s42\n"
      "v_mfma_f32_16x16x32_bf16 a[28:31], a[148:151], v[60:63], a[28:31]\n"
      "v_cndmask_b32_dpp v78, v94, v243, vcc row_ror:8 row_mask:0xf bank_mask:0xf\n"
      "v_mfma_f32_16x16x32_bf16 a[32:35], a[152:155], v[48:51], a[32:35]\n"
      "ds_read_b128 a[140:143], v237\n"
      "v_mov_b32_dpp v94, v243 row_ror:8 row_mask:0xf bank_mask:0x3\n"
      "v_perm_b32 v241, v220, v216, s43\n"
      "v_mfma_f32_16x16x32_bf16 a[36:39], a[152:155], v[52:55], a[36:39]\n"
      "buffer_load_dwordx4 v[164:167], v10, s[68:71], s40 offen offset:1024\n"
      "v_mfma_f32_16x16x32_bf16 a[40:43], a[152:155], v[56:59], a[40:43]\n"
      "v_perm_b32 v87, v222, v218, s43\n"
      "v_cndmask_b32_dpp v67, v83, v240, vcc row_ror:8 row_mask:0xf bank_mask:0xf\n"
      "v_mfma_f32_16x16x32_bf16 a[44:47], a[152:155], v[60:63], a[44:47]\n"
      "v_mov_b32_dpp v83, v240 row_ror:8 row_mask:0xf bank_mask:0x3\n"
      "v_mfma_f32_16x16x32_bf16 a[48:51], a[156:159], v[48:51], a[48:51]\n"
      "ds_read_b128 a[144:147], v238\n"
      "v_perm_b32 v242, v221, v217, s42\n"
      "v_perm_b32 v91, v223, v219, s42\n"
      "v_mfma_f32_16x16x32_bf16 a[52:55], a[156:159], v[52:55], a[52:55]\n"
      "v_cndmask_b32_dpp v71, v87, v241, vcc row_ror:8 row_mask:0xf bank_mask:0xf\n"
      "v_mfma_f32_16x16x32_bf16 a[56:59], a[156:159], v[56:59], a[56:59]\n"
      "buffer_load_dwordx4 v[168:171], v10, s[68:71], s40 offen offset:2048\n"
      "v_mfma_f32_16x16x32_bf16 a[60:63], a[156:159], v[60:63], a[60:63]\n"
      "v_mov_b32_dpp v87, v241 row_ror:8 row_mask:0xf bank_mask:0x3\n"
      "v_perm_b32 v243, v221, v217, s43\n"
      "s_waitcnt lgkmcnt(15)\n"
      "v_mfma_f32_16x16x32_bf16 a[64:67], a[160:163], v[48:51], a[64:67]\n"
      "ds_read_b128 a[148:151], v239\n"
      "v_perm_b32 v95, v223, v219, s43\n"
      "v_cndmask_b32_dpp v75, v91, v242, vcc row_ror:8 row_mask:0xf bank_mask:0xf\n"
      "v_mfma_f32_16x16x32_bf16 a[68:71], a[160:163], v[52:55], a[68:71]\n"
      "v_mov_b32_dpp v91, v242 row_ror:8 row_mask:0xf bank_mask:0x3\n"
      "v_mfma_f32_16x16x32_bf16 a[72:75], a[160:163], v[56:59], a[72:75]\n"
      "ds_read_b128 a[152:155], v232 offset:16\n"
      "v_cndmask_b32_dpp v79, v95, v243, vcc row_ror:8 row_mask:0xf bank_mask:0xf\n"
      "v_mfma_f32_16x16x32_bf16 a[76:79], a[160:163], v[60:63], a[76:79]\n"
      "buffer_load_dwordx4 v[172:175], v10, s[68:71], s40 offen offset:3072\n"
      "v_mfma_f32_16x16x32_bf16 a[80:83], a[164:167], v[48:51], a[80:83]\n"
      "ds_read_b128 a[156:159], v233 offset:16\n"
      "s_mov_b32 s40, 4214784\n"
      "v_mov_b32_dpp v95, v243 row_ror:8 row_mask:0xf bank_mask:0x3\n"
      "v_mfma_f32_16x16x32_bf16 a[84:87], a[164:167], v[52:55], a[84:87]\n"
      "v_mfma_f32_16x16x32_bf16 a[88:91], a[164:167], v[56:59], a[88:91]\n"
      "v_mfma_f32_16x16x32_bf16 a[92:95], a[164:167], v[60:63], a[92:95]\n"
      "v_mfma_f32_16x16x32_bf16 a[96:99], a[168:171], v[48:51], a[96:99]\n"
      "ds_read_b128 a[160:163], v234 offset:16\n"
      "v_mfma_f32_16x16x32_bf16 a[100:103], a[168:171], v[52:55], a[100:103]\n"
      "buffer_load_dwordx4 v[176:179], v10, s[68:71], s40 offen\n"
      "v_mfma_f32_16x16x32_bf16 a[104:107], a[168:171], v[56:59], a[104:107]\n"
      "s_waitcnt lgkmcnt(15)\n"
      "v_mul_u32_u24 v224, s46, v224\n"
      "v_mfma_f32_16x16x32_bf16 a[108:111], a[168:171], v[60:63], a[108:111]\n"
      "v_mfma_f32_16x16x32_bf16 a[112:115], a[172:175], v[48:51], a[112:115]\n"
      "ds_read_b128 a[164:167], v235 offset:16\n"
      "v_mfma_f32_16x16x32_bf16 a[116:119], a[172:175], v[52:55], a[116:119]\n"
      "v_mul_u32_u24 v225, s46, v225\n"
      "v_mfma_f32_16x16x32_bf16 a[120:123], a[172:175], v[56:59], a[120:123]\n"
      "buffer_load_dwordx4 v[180:183], v10, s[68:71], s40 offen offset:1024\n"
      "v_mfma_f32_16x16x32_bf16 a[124:127], a[172:175], v[60:63], a[124:127]\n"
      "v_mfma_f32_16x16x32_bf16 a[0:3], a[176:179], v[64:67], a[0:3]\n"
      "ds_read_b128 a[168:171], v236 offset:16\n"
      "s_waitcnt vmcnt(21)\n"
      "v_perm_b32 v240, v100, v96, s42\n"
      "v_mfma_f32_16x16x32_bf16 a[4:7], a[176:179], v[68:71], a[4:7]\n"
      "v_mul_u32_u24 v226, s46, v226\n"
      "v_perm_b32 v48, v102, v98, s42\n"
      "v_mfma_f32_16x16x32_bf16 a[8:11], a[176:179], v[72:75], a[8:11]\n"
      "s_waitcnt lgkmcnt(15)\n"
      "v_mul_u32_u24 v227, s46, v227\n"
      "v_perm_b32 v241, v100, v96, s43\n"
      "v_mfma_f32_16x16x32_bf16 a[12:15], a[176:179], v[76:79], a[12:15]\n"
      "buffer_load_dwordx4 v[184:187], v10, s[68:71], s40 offen offset:2048\n"
      "v_mfma_f32_16x16x32_bf16 a[16:19], a[180:183], v[64:67], a[16:19]\n"
      "ds_read_b128 a[172:175], v237 offset:16\n"
      "v_perm_b32 v52, v102, v98, s43\n"
      "v_cndmask_b32_dpp v32, v48, v240, vcc row_ror:8 row_mask:0xf bank_mask:0xf\n"
      "v_mfma_f32_16x16x32_bf16 a[20:23], a[180:183], v[68:71], a[20:23]\n"
      "s_waitcnt lgkmcnt(14)\n"
      "v_mul_u32_u24 v228, s46, v228\n"
      "v_mov_b32_dpp v48, v240 row_ror:8 row_mask:0xf bank_mask:0x3\n"
      "v_mfma_f32_16x16x32_bf16 a[24:27], a[180:183], v[72:75], a[24:27]\n"
      "s_waitcnt lgkmcnt(12)\n"
      "v_mul_u32_u24 v229, s46, v229\n"
      "v_perm_b32 v242, v101, v97, s42\n"
      "v_mfma_f32_16x16x32_bf16 a[28:31], a[180:183], v[76:79], a[28:31]\n"
      "v_perm_b32 v56, v103, v99, s42\n"
      "v_cndmask_b32_dpp v36, v52, v241, vcc row_ror:8 row_mask:0xf bank_mask:0xf\n"
      "v_mfma_f32_16x16x32_bf16 a[32:35], a[128:131], v[64:67], a[32:35]\n"
      "ds_read_b128 a[176:179], v238 offset:16\n"
      "v_mov_b32_dpp v52, v241 row_ror:8 row_mask:0xf bank_mask:0x3\n"
      "v_perm_b32 v243, v101, v97, s43\n"
      "v_mfma_f32_16x16x32_bf16 a[36:39], a[128:131], v[68:71], a[36:39]\n"
      "buffer_load_dwordx4 v[188:191], v10, s[68:71], s40 offen offset:3072\n"
      "v_mfma_f32_16x16x32_bf16 a[40:43], a[128:131], v[72:75], a[40:43]\n"
      "s_mov_b32 s40, 6291456\n"
      "s_waitcnt lgkmcnt(11)\n"
      "v_mul_u32_u24 v230, s46, v230\n"
      "v_perm_b32 v60, v103, v99, s43\n"
      "v_mfma_f32_16x16x32_bf16 a[44:47], a[128:131], v[76:79], a[44:47]\n"
      "v_cndmask_b32_dpp v40, v56, v242, vcc row_ror:8 row_mask:0xf bank_mask:0xf\n"
      "v_mov_b32_dpp v56, v242 row_ror:8 row_mask:0xf bank_mask:0x3\n"
      "v_mfma_f32_16x16x32_bf16 a[48:51], a[132:135], v[64:67], a[48:51]\n"
      "ds_read_b128 a[180:183], v239 offset:16\n"
      "s_waitcnt vmcnt(21)\n"
      "v_perm_b32 v240, v108, v104, s42\n"
      "v_mfma_f32_16x16x32_bf16 a[52:55], a[132:135], v[68:71], a[52:55]\n"
      "s_waitcnt lgkmcnt(11)\n"
      "v_mul_u32_u24 v231, s46, v231\n"
      "v_perm_b32 v49, v110, v106, s42\n"
      "v_mfma_f32_16x16x32_bf16 a[56:59], a[132:135], v[72:75], a[56:59]\n"
      "buffer_load_dwordx4 v[192:195], v10, s[68:71], s40 offen\n"
      "v_mfma_f32_16x16x32_bf16 a[60:63], a[132:135], v[76:79], a[60:63]\n"
      "v_cndmask_b32_dpp v44, v60, v243, vcc row_ror:8 row_mask:0xf bank_mask:0xf\n"
      "v_mov_b32_dpp v60, v243 row_ror:8 row_mask:0xf bank_mask:0x3\n"
      "v_mfma_f32_16x16x32_bf16 a[64:67], a[136:139], v[64:67], a[64:67]\n"
      "ds_read_b128 a[128:131], v232 offset:32\n"
      "v_perm_b32 v241, v108, v104, s43\n"
      "v_perm_b32 v53, v110, v106, s43\n"
      "v_mfma_f32_16x16x32_bf16 a[68:71], a[136:139], v[68:71], a[68:71]\n"
      "v_and_b32 v224, -16, v224\n"
      "v_cndmask_b32_dpp v33, v49, v240, vcc row_ror:8 row_mask:0xf bank_mask:0xf\n"
      "v_mfma_f32_16x16x32_bf16 a[72:75], a[136:139], v[72:75], a[72:75]\n"
      "v_and_b32 v225, -16, v225\n"
      "v_mov_b32_dpp v49, v240 row_ror:8 row_mask:0xf bank_mask:0x3\n"
      "v_mfma_f32_16x16x32_bf16 a[76:79], a[136:139], v[76:79], a[76:79]\n"
      "v_perm_b32 v242, v109, v105, s42\n"
      "v_perm_b32 v57, v111, v107, s42\n"
      "s_waitcnt lgkmcnt(8)\n"
      "v_mfma_f32_16x16x32_bf16 a[80:83], a[140:143], v[64:67], a[80:83]\n"
      "ds_read_b128 a[132:135], v233 offset:32\n"
      "v_cndmask_b32_dpp v37, v53, v241, vcc row_ror:8 row_mask:0xf bank_mask:0xf\n"
      "v_mfma_f32_16x16x32_bf16 a[84:87], a[140:143], v[68:71], a[84:87]\n"
      "buffer_load_dwordx4 v[196:199], v10, s[68:71], s40 offen offset:1024\n"
      "v_mfma_f32_16x16x32_bf16 a[88:91], a[140:143], v[72:75], a[88:91]\n"
      "v_and_b32 v226, -16, v226\n"
      "v_mov_b32_dpp v53, v241 row_ror:8 row_mask:0xf bank_mask:0x3\n"
      "v_mfma_f32_16x16x32_bf16 a[92:95], a[140:143], v[76:79], a[92:95]\n"
      "v_perm_b32 v243, v109, v105, s43\n"
      "v_perm_b32 v61, v111, v107, s43\n"
      "v_mfma_f32_16x16x32_bf16 a[96:99], a[144:147], v[64:67], a[96:99]\n"
      "ds_read_b128 a[136:139], v234 offset:32\n"
      "v_cndmask_b32_dpp v41, v57, v242, vcc row_ror:8 row_mask:0xf bank_mask:0xf\n"
      "v_mov_b32_dpp v57, v242 row_ror:8 row_mask:0xf bank_mask:0x3\n"
      "v_mfma_f32_16x16x32_bf16 a[100:103], a[144:147], v[68:71], a[100:103]\n"
      "v_and_b32 v227, -16, v227\n"
      "s_waitcnt vmcnt(21)\n"
      "v_perm_b32 v240, v116, v112, s42\n"
      "v_mfma_f32_16x16x32_bf16 a[104:107], a[144:147], v[72:75], a[104:107]\n"
      "buffer_load_dwordx4 v[200:203], v10, s[68:71], s40 offen offset:2048\n"
      "v_mfma_f32_16x16x32_bf16 a[108:111], a[144:147], v[76:79], a[108:111]\n"
      "v_perm_b32 v50, v118, v114, s42\n"
      "v_cndmask_b32_dpp v45, v61, v243, vcc row_ror:8 row_mask:0xf bank_mask:0xf\n"
      "v_mfma_f32_16x16x32_bf16 a[112:115], a[148:151], v[64:67], a[112:115]\n"
      "ds_read_b128 a[140:143], v235 offset:32\n"
      "v_mov_b32_dpp v61, v243 row_ror:8 row_mask:0xf bank_mask:0x3\n"
      "v_perm_b32 v241, v116, v112, s43\n"
      "v_mfma_f32_16x16x32_bf16 a[116:119], a[148:151], v[68:71], a[116:119]\n"
      "v_and_b32 v228, -16, v228\n"
      "v_perm_b32 v54, v118, v114, s43\n"
      "v_mfma_f32_16x16x32_bf16 a[120:123], a[148:151], v[72:75], a[120:123]\n"
      "v_and_b32 v229, -16, v229\n"
      "v_cndmask_b32_dpp v34, v50, v240, vcc row_ror:8 row_mask:0xf bank_mask:0xf\n"
      "v_mfma_f32_16x16x32_bf16 a[124:127], a[148:151], v[76:79], a[124:127]\n"
      "buffer_load_dwordx4 v[204:207], v10, s[68:71], s40 offen offset:3072\n"
      "v_mfma_f32_16x16x32_bf16 a[0:3], a[152:155], v[80:83], a[0:3]\n"
      "ds_read_b128 a[144:147], v236 offset:32\n"
      "s_mov_b32 s40, 6295552\n"
      "v_mov_b32_dpp v50, v240 row_ror:8 row_mask:0xf bank_mask:0x3\n"
      "v_mfma_f32_16x16x32_bf16 a[4:7], a[152:155], v[84:87], a[4:7]\n"
      "v_and_b32 v230, -16, v230\n"
      "v_perm_b32 v242, v117, v113, s42\n"
      "v_mfma_f32_16x16x32_bf16 a[8:11], a[152:155], v[88:91], a[8:11]\n"
      "v_and_b32 v231, -16, v231\n"
      "v_perm_b32 v58, v119, v115, s42\n"
      "v_mfma_f32_16x16x32_bf16 a[12:15], a[152:155], v[92:95], a[12:15]\n"
      "v_cndmask_b32_dpp v38, v54, v241, vcc row_ror:8 row_mask:0xf bank_mask:0xf\n"
      "v_mov_b32_dpp v54, v241 row_ror:8 row_mask:0xf bank_mask:0x3\n"
      "s_waitcnt lgkmcnt(8)\n"
      "v_mfma_f32_16x16x32_bf16 a[16:19], a[156:159], v[80:83], a[16:19]\n"
      "ds_read_b128 a[148:151], v237 offset:32\n"
      "v_perm_b32 v243, v117, v113, s43\n"
      "v_perm_b32 v62, v119, v115, s43\n"
      "v_mfma_f32_16x16x32_bf16 a[20:23], a[156:159], v[84:87], a[20:23]\n"
      "buffer_load_dwordx4 v[208:211], v10, s[68:71], s40 offen\n"
      "v_mfma_f32_16x16x32_bf16 a[24:27], a[156:159], v[88:91], a[24:27]\n"
      "v_cndmask_b32_dpp v42, v58, v242, vcc row_ror:8 row_mask:0xf bank_mask:0xf\n"
      "v_mov_b32_dpp v58, v242 row_ror:8 row_mask:0xf bank_mask:0x3\n"
      "v_mfma_f32_16x16x32_bf16 a[28:31], a[156:159], v[92:95], a[28:31]\n"
      "s_waitcnt vmcnt(22)\n"
      "v_perm_b32 v240, v124, v120, s42\n"
      "v_perm_b32 v51, v126, v122, s42\n"
      "v_mfma_f32_16x16x32_bf16 a[32:35], a[160:163], v[80:83], a[32:35]\n"
      "ds_read_b128 a[152:155], v238 offset:32\n"
      "v_cndmask_b32_dpp v46, v62, v243, vcc row_ror:8 row_mask:0xf bank_mask:0xf\n"
      "v_mfma_f32_16x16x32_bf16 a[36:39], a[160:163], v[84:87], a[36:39]\n"
      "v_mov_b32_dpp v62, v243 row_ror:8 row_mask:0xf bank_mask:0x3\n"
      "v_mfma_f32_16x16x32_bf16 a[40:43], a[160:163], v[88:91], a[40:43]\n"
      "buffer_load_dwordx4 v[212:215], v10, s[68:71], s40 offen offset:1024\n"
      "v_mfma_f32_16x16x32_bf16 a[44:47], a[160:163], v[92:95], a[44:47]\n"
      "v_perm_b32 v241, v124, v120, s43\n"
      "v_perm_b32 v55, v126, v122, s43\n"
      "v_mfma_f32_16x16x32_bf16 a[48:51], a[164:167], v[80:83], a[48:51]\n"
      "ds_read_b128 a[156:159], v239 offset:32\n"
      "v_cndmask_b32_dpp v35, v51, v240, vcc row_ror:8 row_mask:0xf bank_mask:0xf\n"
      "v_mov_b32_dpp v51, v240 row_ror:8 row_mask:0xf bank_mask:0x3\n"
      "v_mfma_f32_16x16x32_bf16 a[52:55], a[164:167], v[84:87], a[52:55]\n"
      "v_perm_b32 v242, v125, v121, s42\n"
      "v_mfma_f32_16x16x32_bf16 a[56:59], a[164:167], v[88:91], a[56:59]\n"
      "v_perm_b32 v59, v127, v123, s42\n"
      "v_mfma_f32_16x16x32_bf16 a[60:63], a[164:167], v[92:95], a[60:63]\n"
      "buffer_load_dwordx4 v[216:219], v10, s[68:71], s40 offen offset:2048\n"
      "v_mfma_f32_16x16x32_bf16 a[64:67], a[168:171], v[80:83], a[64:67]\n"
      "ds_read_b128 a[160:163], v232 offset:48\n"
      "v_cndmask_b32_dpp v39, v55, v241, vcc row_ror:8 row_mask:0xf bank_mask:0xf\n"
      "v_mov_b32_dpp v55, v241 row_ror:8 row_mask:0xf bank_mask:0x3\n"
      "v_mfma_f32_16x16x32_bf16 a[68:71], a[168:171], v[84:87], a[68:71]\n"
      "v_perm_b32 v243, v125, v121, s43\n"
      "v_perm_b32 v63, v127, v123, s43\n"
      "v_mfma_f32_16x16x32_bf16 a[72:75], a[168:171], v[88:91], a[72:75]\n"
      "v_cndmask_b32_dpp v43, v59, v242, vcc row_ror:8 row_mask:0xf bank_mask:0xf\n"
      "v_mfma_f32_16x16x32_bf16 a[76:79], a[168:171], v[92:95], a[76:79]\n"
      "v_mov_b32_dpp v59, v242 row_ror:8 row_mask:0xf bank_mask:0x3\n"
      "s_waitcnt lgkmcnt(8)\n"
      "v_mfma_f32_16x16x32_bf16 a[80:83], a[172:175], v[80:83], a[80:83]\n"
      "ds_read_b128 a[164:167], v233 offset:48\n"
      "v_cndmask_b32_dpp v47, v63, v243, vcc row_ror:8 row_mask:0xf bank_mask:0xf\n"
      "v_mov_b32_dpp v63, v243 row_ror:8 row_mask:0xf bank_mask:0x3\n"
      "v_mfma_f32_16x16x32_bf16 a[84:87], a[172:175], v[84:87], a[84:87]\n"
      "buffer_load_dwordx4 v[220:223], v10, s[68:71], s40 offen offset:3072\n"
      "v_mfma_f32_16x16x32_bf16 a[88:91], a[172:175], v[88:91], a[88:91]\n"
      "s_mov_b32 s40, 6307840\n"
      "v_mfma_f32_16x16x32_bf16 a[92:95], a[172:175], v[92:95], a[92:95]\n"
      "v_mfma_f32_16x16x32_bf16 a[96:99], a[176:179], v[80:83], a[96:99]\n"
      "ds_read_b128 a[168:171], v234 offset:48\n"
      "v_mfma_f32_16x16x32_bf16 a[100:103], a[176:179], v[84:87], a[100:103]\n"
      "v_mfma_f32_16x16x32_bf16 a[104:107], a[176:179], v[88:91], a[104:107]\n"
      "buffer_load_dwordx4 v[96:99], v10, s[68:71], s40 offen\n"
      "v_mfma_f32_16x16x32_bf16 a[108:111], a[176:179], v[92:95], a[108:111]\n"
      "v_mfma_f32_16x16x32_bf16 a[112:115], a[180:183], v[80:83], a[112:115]\n"
      "ds_read_b128 a[172:175], v235 offset:48\n"
      "v_mfma_f32_16x16x32_bf16 a[116:119], a[180:183], v[84:87], a[116:119]\n"
      "v_mfma_f32_16x16x32_bf16 a[120:123], a[180:183], v[88:91], a[120:123]\n"
      "v_mfma_f32_16x16x32_bf16 a[124:127], a[180:183], v[92:95], a[124:127]\n"
      "buffer_load_dwordx4 v[100:103], v10, s[68:71], s40 offen offset:1024\n"
      "v_mfma_f32_16x16x32_bf16 a[0:3], a[128:131], v[32:35], a[0:3]\n"
      "ds_read_b128 a[176:179], v236 offset:48\n"
      "s_waitcnt vmcnt(25)\n"
      "v_perm_b32 v240, v132, v128, s42\n"
      "v_mfma_f32_16x16x32_bf16 a[4:7], a[128:131], v[36:39], a[4:7]\n"
      "v_perm_b32 v80, v134, v130, s42\n"
      "v_perm_b32 v241, v132, v128, s43\n"
      "v_mfma_f32_16x16x32_bf16 a[8:11], a[128:131], v[40:43], a[8:11]\n"
      "v_perm_b32 v84, v134, v130, s43\n"
      "v_cndmask_b32_dpp v64, v80, v240, vcc row_ror:8 row_mask:0xf bank_mask:0xf\n"
      "v_mfma_f32_16x16x32_bf16 a[12:15], a[128:131], v[44:47], a[12:15]\n"
      "v_mov_b32_dpp v80, v240 row_ror:8 row_mask:0xf bank_mask:0x3\n"
      "s_waitcnt lgkmcnt(8)\n"
      "v_mfma_f32_16x16x32_bf16 a[16:19], a[132:135], v[32:35], a[16:19]\n"
      "ds_read_b128 a[180:183], v237 offset:48\n"
      "v_perm_b32 v242, v133, v129, s42\n"
      "v_mfma_f32_16x16x32_bf16 a[20:23], a[132:135], v[36:39], a[20:23]\n"
      "buffer_load_dwordx4 v[104:107], v10, s[68:71], s40 offen offset:2048\n"
      "v_mfma_f32_16x16x32_bf16 a[24:27], a[132:135], v[40:43], a[24:27]\n"
      "v_perm_b32 v88, v135, v131, s42\n"
      "v_cndmask_b32_dpp v68, v84, v241, vcc row_ror:8 row_mask:0xf bank_mask:0xf\n"
      "v_mfma_f32_16x16x32_bf16 a[28:31], a[132:135], v[44:47], a[28:31]\n"
      "v_mov_b32_dpp v84, v241 row_ror:8 row_mask:0xf bank_mask:0x3\n"
      "v_perm_b32 v243, v133, v129, s43\n"
      "v_mfma_f32_16x16x32_bf16 a[32:35], a[136:139], v[32:35], a[32:35]\n"
      "ds_read_b128 a[128:131], v238 offset:48\n"
      "v_perm_b32 v92, v135, v131, s43\n"
      "v_mfma_f32_16x16x32_bf16 a[36:39], a[136:139], v[36:39], a[36:39]\n"
      "v_cndmask_b32_dpp v72, v88, v242, vcc row_ror:8 row_mask:0xf bank_mask:0xf\n"
      "v_mfma_f32_16x16x32_bf16 a[40:43], a[136:139], v[40:43], a[40:43]\n"
      "buffer_load_dwordx4 v[108:111], v10, s[68:71], s40 offen offset:3072\n"
      "v_mfma_f32_16x16x32_bf16 a[44:47], a[136:139], v[44:47], a[44:47]\n"
      "s_mov_b32 s40, 6311936\n"
      "v_mov_b32_dpp v88, v242 row_ror:8 row_mask:0xf bank_mask:0x3\n"
      "s_waitcnt vmcnt(24)\n"
      "v_perm_b32 v240, v140, v136, s42\n"
      "v_mfma_f32_16x16x32_bf16 a[48:51], a[140:143], v[32:35], a[48:51]\n"
      "ds_read_b128 a[132:135], v239 offset:48\n"
      "v_perm_b32 v81, v142, v138, s42\n"
      "v_cndmask_b32_dpp v76, v92, v243, vcc row_ror:8 row_mask:0xf bank_mask:0xf\n"
      "v_mfma_f32_16x16x32_bf16 a[52:55], a[140:143], v[36:39], a[52:55]\n"
      "ds_read_b32 v232, v12 offset:192\n"
      "v_mov_b32_dpp v92, v243 row_ror:8 row_mask:0xf bank_mask:0x3\n"
      "v_mfma_f32_16x16x32_bf16 a[56:59], a[140:143], v[40:43], a[56:59]\n"
      "ds_read_b32 v233, v13 offset:192\n"
      "v_perm_b32 v241, v140, v136, s43\n"
      "v_mfma_f32_16x16x32_bf16 a[60:63], a[140:143], v[44:47], a[60:63]\n"
      "buffer_load_dwordx4 v[112:115], v10, s[68:71], s40 offen\n"
      "v_mfma_f32_16x16x32_bf16 a[64:67], a[144:147], v[32:35], a[64:67]\n"
      "ds_read_b128 a[136:139], v224\n"
      "v_perm_b32 v85, v142, v138, s43\n"
      "v_cndmask_b32_dpp v65, v81, v240, vcc row_ror:8 row_mask:0xf bank_mask:0xf\n"
      "v_mfma_f32_16x16x32_bf16 a[68:71], a[144:147], v[36:39], a[68:71]\n"
      "ds_read_b32 v234, v14 offset:192\n"
      "v_mov_b32_dpp v81, v240 row_ror:8 row_mask:0xf bank_mask:0x3\n"
      "v_perm_b32 v242, v141, v137, s42\n"
      "v_mfma_f32_16x16x32_bf16 a[72:75], a[144:147], v[40:43], a[72:75]\n"
      "ds_read_b32 v235, v15 offset:192\n"
      "v_perm_b32 v89, v143, v139, s42\n"
      "v_mfma_f32_16x16x32_bf16 a[76:79], a[144:147], v[44:47], a[76:79]\n"
      "v_cndmask_b32_dpp v69, v85, v241, vcc row_ror:8 row_mask:0xf bank_mask:0xf\n"
      "s_waitcnt lgkmcnt(12)\n"
      "v_mfma_f32_16x16x32_bf16 a[80:83], a[148:151], v[32:35], a[80:83]\n"
      "ds_read_b128 a[140:143], v225\n"
      "v_mov_b32_dpp v85, v241 row_ror:8 row_mask:0xf bank_mask:0x3\n"
      "v_perm_b32 v243, v141, v137, s43\n"
      "v_mfma_f32_16x16x32_bf16 a[84:87], a[148:151], v[36:39], a[84:87]\n"
      "buffer_load_dwordx4 v[116:119], v10, s[68:71], s40 offen offset:1024\n"
      "v_mfma_f32_16x16x32_bf16 a[88:91], a[148:151], v[40:43], a[88:91]\n"
      "ds_read_b32 v236, v12 offset:25792\n"
      "v_perm_b32 v93, v143, v139, s43\n"
      "v_cndmask_b32_dpp v73, v89, v242, vcc row_ror:8 row_mask:0xf bank_mask:0xf\n"
      "v_mfma_f32_16x16x32_bf16 a[92:95], a[148:151], v[44:47], a[92:95]\n"
      "v_mov_b32_dpp v89, v242 row_ror:8 row_mask:0xf bank_mask:0x3\n"
      "v_mfma_f32_16x16x32_bf16 a[96:99], a[152:155], v[32:35], a[96:99]\n"
      "ds_read_b128 a[144:147], v226\n"
      "s_waitcnt vmcnt(24)\n"
      "v_perm_b32 v240, v148, v144, s42\n"
      "v_mfma_f32_16x16x32_bf16 a[100:103], a[152:155], v[36:39], a[100:103]\n"
      "ds_read_b32 v237, v13 offset:25792\n"
      "v_perm_b32 v82, v150, v146, s42\n"
      "v_cndmask_b32_dpp v77, v93, v243, vcc row_ror:8 row_mask:0xf bank_mask:0xf\n"
      "v_mfma_f32_16x16x32_bf16 a[104:107], a[152:155], v[40:43], a[104:107]\n"
      "buffer_load_dwordx4 v[120:123], v10, s[68:71], s40 offen offset:2048\n"
      "v_mfma_f32_16x16x32_bf16 a[108:111], a[152:155], v[44:47], a[108:111]\n"
      "v_mov_b32_dpp v93, v243 row_ror:8 row_mask:0xf bank_mask:0x3\n"
      "v_perm_b32 v241, v148, v144, s43\n"
      "v_mfma_f32_16x16x32_bf16 a[112:115], a[156:159], v[32:35], a[112:115]\n"
      "ds_read_b128 a[148:151], v227\n"
      "v_perm_b32 v86, v150, v146, s43\n"
      "v_cndmask_b32_dpp v66, v82, v240, vcc row_ror:8 row_mask:0xf bank_mask:0xf\n"
      "v_mfma_f32_16x16x32_bf16 a[116:119], a[156:159], v[36:39], a[116:119]\n"
      "ds_read_b32 v238, v14 offset:25792\n"
      "v_mov_b32_dpp v82, v240 row_ror:8 row_mask:0xf bank_mask:0x3\n"
      "v_mfma_f32_16x16x32_bf16 a[120:123], a[156:159], v[40:43], a[120:123]\n"
      "ds_read_b32 v239, v15 offset:25792\n"
      "v_perm_b32 v242, v149, v145, s42\n"
      "v_mfma_f32_16x16x32_bf16 a[124:127], a[156:159], v[44:47], a[124:127]\n"
      "buffer_load_dwordx4 v[124:127], v10, s[68:71], s40 offen offset:3072\n"
      "v_mfma_f32_16x16x32_bf16 a[0:3], a[160:163], v[48:51], a[0:3]\n"
      "ds_read_b128 a[152:155], v228\n"
      "s_mov_b32 s40, 8388608\n"
      "v_perm_b32 v90, v151, v147, s42\n"
      "v_mfma_f32_16x16x32_bf16 a[4:7], a[160:163], v[52:55], a[4:7]\n"
      "v_cndmask_b32_dpp v70, v86, v241, vcc row_ror:8 row_mask:0xf bank_mask:0xf\n"
      "v_mov_b32_dpp v86, v241 row_ror:8 row_mask:0xf bank_mask:0x3\n"
      "v_mfma_f32_16x16x32_bf16 a[8:11], a[160:163], v[56:59], a[8:11]\n"
      "v_perm_b32 v243, v149, v145, s43\n"
      "v_perm_b32 v94, v151, v147, s43\n"
      "v_mfma_f32_16x16x32_bf16 a[12:15], a[160:163], v[60:63], a[12:15]\n"
      "v_cndmask_b32_dpp v74, v90, v242, vcc row_ror:8 row_mask:0xf bank_mask:0xf\n"
      "s_waitcnt lgkmcnt(15)\n"
      "v_mfma_f32_16x16x32_bf16 a[16:19], a[164:167], v[48:51], a[16:19]\n"
      "ds_read_b128 a[156:159], v229\n"
      "v_mov_b32 v244, -1\n"
      "v_mov_b32_dpp v90, v242 row_ror:8 row_mask:0xf bank_mask:0x3\n"
      "v_mfma_f32_16x16x32_bf16 a[20:23], a[164:167], v[52:55], a[20:23]\n"
      "buffer_load_dwordx4 v[128:131], v10, s[68:71], s40 offen\n"
      "v_mfma_f32_16x16x32_bf16 a[24:27], a[164:167], v[56:59], a[24:27]\n"
      "v_mov_b32 v245, -1\n"
      "s_waitcnt vmcnt(25)\n"
      "v_perm_b32 v240, v156, v152, s42\n"
      "v_mfma_f32_16x16x32_bf16 a[28:31], a[164:167], v[60:63], a[28:31]\n"
      "v_mov_b32 v246, -1\n"
      "v_perm_b32 v83, v158, v154, s42\n"
      "v_mfma_f32_16x16x32_bf16 a[32:35], a[168:171], v[48:51], a[32:35]\n"
      "ds_read_b128 a[160:163], v230\n"
      "v_mov_b32 v247, -1\n"
      "v_cndmask_b32_dpp v78, v94, v243, vcc row_ror:8 row_mask:0xf bank_mask:0xf\n"
      "v_mfma_f32_16x16x32_bf16 a[36:39], a[168:171], v[52:55], a[36:39]\n"
      "v_mov_b32_dpp v94, v243 row_ror:8 row_mask:0xf bank_mask:0x3\n"
      "v_perm_b32 v241, v156, v152, s43\n"
      "v_mfma_f32_16x16x32_bf16 a[40:43], a[168:171], v[56:59], a[40:43]\n"
      "buffer_load_dwordx4 v[132:135], v10, s[68:71], s40 offen offset:1024\n"
      "v_mfma_f32_16x16x32_bf16 a[44:47], a[168:171], v[60:63], a[44:47]\n"
      "v_perm_b32 v87, v158, v154, s43\n"
      "v_cndmask_b32_dpp v67, v83, v240, vcc row_ror:8 row_mask:0xf bank_mask:0xf\n"
      "v_mfma_f32_16x16x32_bf16 a[48:51], a[172:175], v[48:51], a[48:51]\n"
      "ds_read_b128 a[164:167], v231\n"
      "v_mov_b32_dpp v83, v240 row_ror:8 row_mask:0xf bank_mask:0x3\n"
      "v_perm_b32 v242, v157, v153, s42\n"
      "v_mfma_f32_16x16x32_bf16 a[52:55], a[172:175], v[52:55], a[52:55]\n"
      "s_mov_b32 s45, 0\n"
      "buffer_store_dwordx4 v[244:247], v29, s[76:79], s45 offen sc1\n"
      "v_mfma_f32_16x16x32_bf16 a[56:59], a[172:175], v[56:59], a[56:59]\n"
      "v_perm_b32 v91, v159, v155, s42\n"
      "v_cndmask_b32_dpp v71, v87, v241, vcc row_ror:8 row_mask:0xf bank_mask:0xf\n"
      "v_mfma_f32_16x16x32_bf16 a[60:63], a[172:175], v[60:63], a[60:63]\n"
      "buffer_load_dwordx4 v[136:139], v10, s[68:71], s40 offen offset:2048\n"
      "v_mfma_f32_16x16x32_bf16 a[64:67], a[176:179], v[48:51], a[64:67]\n"
      "ds_read_b128 a[168:171], v224 offset:16\n"
      "v_mov_b32_dpp v87, v241 row_ror:8 row_mask:0xf bank_mask:0x3\n"
      "v_perm_b32 v243, v157, v153, s43\n"
      "v_mfma_f32_16x16x32_bf16 a[68:71], a[176:179], v[52:55], a[68:71]\n"
      "s_waitcnt lgkmcnt(15)\n"
      "v_mul_u32_u24 v232, s46, v232\n"
      "v_perm_b32 v95, v159, v155, s43\n"
      "v_mfma_f32_16x16x32_bf16 a[72:75], a[176:179], v[56:59], a[72:75]\n"
      "v_mul_u32_u24 v233, s46, v233\n"
      "v_cndmask_b32_dpp v75, v91, v242, vcc row_ror:8 row_mask:0xf bank_mask:0xf\n"
      "v_mfma_f32_16x16x32_bf16 a[76:79], a[176:179], v[60:63], a[76:79]\n"
      "v_mov_b32_dpp v91, v242 row_ror:8 row_mask:0xf bank_mask:0x3\n"
      "v_cndmask_b32_dpp v79, v95, v243, vcc row_ror:8 row_mask:0xf bank_mask:0xf\n"
      "v_mfma_f32_16x16x32_bf16 a[80:83], a[180:183], v[48:51], a[80:83]\n"
      "ds_read_b128 a[172:175], v225 offset:16\n"
      "v_mov_b32_dpp v95, v243 row_ror:8 row_mask:0xf bank_mask:0x3\n"
      "v_mfma_f32_16x16x32_bf16 a[84:87], a[180:183], v[52:55], a[84:87]\n"
      "buffer_load_dwordx4 v[140:143], v10, s[68:71], s40 offen offset:3072\n"
      "v_mfma_f32_16x16x32_bf16 a[88:91], a[180:183], v[56:59], a[88:91]\n"
      "s_mov_b32 s40, 8392704\n"
      "s_waitcnt lgkmcnt(14)\n"
      "v_mul_u32_u24 v234, s46, v234\n"
      "v_mfma_f32_16x16x32_bf16 a[92:95], a[180:183], v[60:63], a[92:95]\n"
      "v_mfma_f32_16x16x32_bf16 a[96:99], a[128:131], v[48:51], a[96:99]\n"
      "ds_read_b128 a[176:179], v226 offset:16\n"
      "v_mfma_f32_16x16x32_bf16 a[100:103], a[128:131], v[52:55], a[100:103]\n"
      "s_waitcnt lgkmcnt(14)\n"
      "v_mul_u32_u24 v235, s46, v235\n"
      "v_mfma_f32_16x16x32_bf16 a[104:107], a[128:131], v[56:59], a[104:107]\n"
      "buffer_load_dwordx4 v[144:147], v10, s[68:71], s40 offen\n"
      "v_mfma_f32_16x16x32_bf16 a[108:111], a[128:131], v[60:63], a[108:111]\n"
      "v_mfma_f32_16x16x32_bf16 a[112:115], a[132:135], v[48:51], a[112:115]\n"
      "ds_read_b128 a[180:183], v227 offset:16\n"
      "v_mfma_f32_16x16x32_bf16 a[116:119], a[132:135], v[52:55], a[116:119]\n"
      "s_waitcnt lgkmcnt(13)\n"
      "v_mul_u32_u24 v236, s46, v236\n"
      "v_mfma_f32_16x16x32_bf16 a[120:123], a[132:135], v[56:59], a[120:123]\n"
      "s_waitcnt lgkmcnt(11)\n"
      "v_mul_u32_u24 v237, s46, v237\n"
      "v_mfma_f32_16x16x32_bf16 a[124:127], a[132:135], v[60:63], a[124:127]\n"
      "buffer_load_dwordx4 v[148:151], v10, s[68:71], s40 offen offset:1024\n"
      "v_mfma_f32_16x16x32_bf16 a[0:3], a[136:139], v[64:67], a[0:3]\n"
      "ds_read_b128 a[128:131], v228 offset:16\n"
      "s_waitcnt vmcnt(29)\n"
      "v_perm_b32 v240, v164, v160, s42\n"
      "v_mfma_f32_16x16x32_bf16 a[4:7], a[136:139], v[68:71], a[4:7]\n"
      "s_waitcnt lgkmcnt(10)\n"
      "v_mul_u32_u24 v238, s46, v238\n"
      "v_perm_b32 v48, v166, v162, s42\n"
      "v_mfma_f32_16x16x32_bf16 a[8:11], a[136:139], v[72:75], a[8:11]\n"
      "s_waitcnt lgkmcnt(9)\n"
      "v_mul_u32_u24 v239, s46, v239\n"
      "v_perm_b32 v241, v164, v160, s43\n"
      "v_mfma_f32_16x16x32_bf16 a[12:15], a[136:139], v[76:79], a[12:15]\n"
      "v_perm_b32 v52, v166, v162, s43\n"
      "v_cndmask_b32_dpp v32, v48, v240, vcc row_ror:8 row_mask:0xf bank_mask:0xf\n"
      "v_mfma_f32_16x16x32_bf16 a[16:19], a[140:143], v[64:67], a[16:19]\n"
      "ds_read_b128 a[132:135], v229 offset:16\n"
      "v_mov_b32_dpp v48, v240 row_ror:8 row_mask:0xf bank_mask:0x3\n"
      "v_perm_b32 v242, v165, v161, s42\n"
      "v_mfma_f32_16x16x32_bf16 a[20:23], a[140:143], v[68:71], a[20:23]\n"
      "buffer_load_dwordx4 v[152:155], v10, s[68:71], s40 offen offset:2048\n"
      "v_mfma_f32_16x16x32_bf16 a[24:27], a[140:143], v[72:75], a[24:27]\n"
      "v_and_b32 v232, -16, v232\n"
      "v_perm_b32 v56, v167, v163, s42\n"
      "v_mfma_f32_16x16x32_bf16 a[28:31], a[140:143], v[76:79], a[28:31]\n"
      "v_cndmask_b32_dpp v36, v52, v241, vcc row_ror:8 row_mask:0xf bank_mask:0xf\n"
      "v_mov_b32_dpp v52, v241 row_ror:8 row_mask:0xf bank_mask:0x3\n"
      "v_mfma_f32_16x16x32_bf16 a[32:35], a[144:147], v[64:67], a[32:35]\n"
      "ds_read_b128 a[136:139], v230 offset:16\n"
      "v_perm_b32 v243, v165, v161, s43\n"
      "v_perm_b32 v60, v167, v163, s43\n"
      "v_mfma_f32_16x16x32_bf16 a[36:39], a[144:147], v[68:71], a[36:39]\n"
      "v_and_b32 v233, -16, v233\n"
      "v_cndmask_b32_dpp v40, v56, v242, vcc row_ror:8 row_mask:0xf bank_mask:0xf\n"
      "v_mfma_f32_16x16x32_bf16 a[40:43], a[144:147], v[72:75], a[40:43]\n"
      "buffer_load_dwordx4 v[156:159], v10, s[68:71], s40 offen offset:3072\n"
      "v_mfma_f32_16x16x32_bf16 a[44:47], a[144:147], v[76:79], a[44:47]\n"
      "s_mov_b32 s40, 8404992\n"
      "v_mov_b32_dpp v56, v242 row_ror:8 row_mask:0xf bank_mask:0x3\n"
      "s_waitcnt vmcnt(29)\n"
      "v_perm_b32 v240, v172, v168, s42\n"
      "v_mfma_f32_16x16x32_bf16 a[48:51], a[148:151], v[64:67], a[48:51]\n"
      "ds_read_b128 a[140:143], v231 offset:16\n"
      "v_perm_b32 v49, v174, v170, s42\n"
      "v_cndmask_b32_dpp v44, v60, v243, vcc row_ror:8 row_mask:0xf bank_mask:0xf\n"
      "v_mfma_f32_16x16x32_bf16 a[52:55], a[148:151], v[68:71], a[52:55]\n"
      "buffer_load_dwordx2 v[250:251], off, s[76:79], s47 sc1\n"
      "v_mfma_f32_16x16x32_bf16 a[56:59], a[148:151], v[72:75], a[56:59]\n"
      "v_and_b32 v234, -16, v234\n"
      "v_mov_b32_dpp v60, v243 row_ror:8 row_mask:0xf bank_mask:0x3\n"
      "v_mfma_f32_16x16x32_bf16 a[60:63], a[148:151], v[76:79], a[60:63]\n"
      "v_perm_b32 v241, v172, v168, s43\n"
      "v_perm_b32 v53, v174, v170, s43\n"
      "s_waitcnt lgkmcnt(8)\n"
      "v_mfma_f32_16x16x32_bf16 a[64:67], a[152:155], v[64:67], a[64:67]\n"
      "ds_read_b128 a[144:147], v224 offset:32\n"
      "v_cndmask_b32_dpp v33, v49, v240, vcc row_ror:8 row_mask:0xf bank_mask:0xf\n"
      "v_mov_b32_dpp v49, v240 row_ror:8 row_mask:0xf bank_mask:0x3\n"
      "v_mfma_f32_16x16x32_bf16 a[68:71], a[152:155], v[68:71], a[68:71]\n"
      "buffer_load_dwordx4 v[160:163], v10, s[68:71], s40 offen\n"
      "v_mfma_f32_16x16x32_bf16 a[72:75], a[152:155], v[72:75], a[72:75]\n"
      "v_and_b32 v235, -16, v235\n"
      "v_perm_b32 v242, v173, v169, s42\n"
      "v_mfma_f32_16x16x32_bf16 a[76:79], a[152:155], v[76:79], a[76:79]\n"
      "v_perm_b32 v57, v175, v171, s42\n"
      "v_cndmask_b32_dpp v37, v53, v241, vcc row_ror:8 row_mask:0xf bank_mask:0xf\n"
      "v_mfma_f32_16x16x32_bf16 a[80:83], a[156:159], v[64:67], a[80:83]\n"
      "ds_read_b128 a[148:151], v225 offset:32\n"
      "v_mov_b32_dpp v53, v241 row_ror:8 row_mask:0xf bank_mask:0x3\n"
      "v_perm_b32 v243, v173, v169, s43\n"
      "v_mfma_f32_16x16x32_bf16 a[84:87], a[156:159], v[68:71], a[84:87]\n"
      "s_mov_b32 s45, 8192\n"
      "buffer_store_dwordx4 v[244:247], v29, s[76:79], s45 offen sc1\n"
      "v_mfma_f32_16x16x32_bf16 a[88:91], a[156:159], v[72:75], a[88:91]\n"
      "v_and_b32 v236, -16, v236\n"
      "v_perm_b32 v61, v175, v171, s43\n"
      "v_mfma_f32_16x16x32_bf16 a[92:95], a[156:159], v[76:79], a[92:95]\n"
      "buffer_load_dwordx4 v[164:167], v10, s[68:71], s40 offen offset:1024\n"
      "v_mfma_f32_16x16x32_bf16 a[96:99], a[160:163], v[64:67], a[96:99]\n"
      "ds_read_b128 a[152:155], v226 offset:32\n"
      "v_cndmask_b32_dpp v41, v57, v242, vcc row_ror:8 row_mask:0xf bank_mask:0xf\n"
      "v_mov_b32_dpp v57, v242 row_ror:8 row_mask:0xf bank_mask:0x3\n"
      "v_mfma_f32_16x16x32_bf16 a[100:103], a[160:163], v[68:71], a[100:103]\n"
      "v_and_b32 v237, -16, v237\n"
      "s_waitcnt vmcnt(31)\n"
      "v_perm_b32 v240, v180, v176, s42\n"
      "v_mfma_f32_16x16x32_bf16 a[104:107], a[160:163], v[72:75], a[104:107]\n"
      "v_and_b32 v238, -16, v238\n"
      "v_perm_b32 v50, v182, v178, s42\n"
      "v_mfma_f32_16x16x32_bf16 a[108:111], a[160:163], v[76:79], a[108:111]\n"
      "v_cndmask_b32_dpp v45, v61, v243, vcc row_ror:8 row_mask:0xf bank_mask:0xf\n"
      "v_mov_b32_dpp v61, v243 row_ror:8 row_mask:0xf bank_mask:0x3\n"
      "v_mfma_f32_16x16x32_bf16 a[112:115], a[164:167], v[64:67], a[112:115]\n"
      "ds_read_b128 a[156:159], v227 offset:32\n"
      "v_perm_b32 v241, v180, v176, s43\n"
      "v_perm_b32 v54, v182, v178, s43\n"
      "v_mfma_f32_16x16x32_bf16 a[116:119], a[164:167], v[68:71], a[116:119]\n"
      "buffer_load_dwordx4 v[168:171], v10, s[68:71], s40 offen offset:2048\n"
      "v_mfma_f32_16x16x32_bf16 a[120:123], a[164:167], v[72:75], a[120:123]\n"
      "v_and_b32 v239, -16, v239\n"
      "v_cndmask_b32_dpp v34, v50, v240, vcc row_ror:8 row_mask:0xf bank_mask:0xf\n"
      "v_mfma_f32_16x16x32_bf16 a[124:127], a[164:167], v[76:79], a[124:127]\n"
      "v_mov_b32_dpp v50, v240 row_ror:8 row_mask:0xf bank_mask:0x3\n"
      "v_perm_b32 v242, v181, v177, s42\n"
      "s_waitcnt lgkmcnt(8)\n"
      "v_mfma_f32_16x16x32_bf16 a[0:3], a[168:171], v[80:83], a[0:3]\n"
      "ds_read_b128 a[160:163], v228 offset:32\n"
      "v_perm_b32 v58, v183, v179, s42\n"
      "v_cndmask_b32_dpp v38, v54, v241, vcc row_ror:8 row_mask:0xf bank_mask:0xf\n"
      "v_mfma_f32_16x16x32_bf16 a[4:7], a[168:171], v[84:87], a[4:7]\n"
      "v_mov_b32_dpp v54, v241 row_ror:8 row_mask:0xf bank_mask:0x3\n"
      "v_mfma_f32_16x16x32_bf16 a[8:11], a[168:171], v[88:91], a[8:11]\n"
      "buffer_load_dwordx4 v[172:175], v10, s[68:71], s40 offen offset:3072\n"
      "v_mfma_f32_16x16x32_bf16 a[12:15], a[168:171], v[92:95], a[12:15]\n"
      "s_mov_b32 s40, 8409088\n"
      "v_perm_b32 v243, v181, v177, s43\n"
      "v_perm_b32 v62, v183, v179, s43\n"
      "v_mfma_f32_16x16x32_bf16 a[16:19], a[172:175], v[80:83], a[16:19]\n"
      "ds_read_b128 a[164:167], v229 offset:32\n"
      "v_cndmask_b32_dpp v42, v58, v242, vcc row_ror:8 row_mask:0xf bank_mask:0xf\n"
      "v_mov_b32_dpp v58, v242 row_ror:8 row_mask:0xf bank_mask:0x3\n"
      "v_mfma_f32_16x16x32_bf16 a[20:23], a[172:175], v[84:87], a[20:23]\n"
      "s_waitcnt vmcnt(31)\n"
      "v_perm_b32 v240, v188, v184, s42\n"
      "v_mfma_f32_16x16x32_bf16 a[24:27], a[172:175], v[88:91], a[24:27]\n"
      "v_perm_b32 v51, v190, v186, s42\n"
      "v_mfma_f32_16x16x32_bf16 a[28:31], a[172:175], v[92:95], a[28:31]\n"
      "buffer_load_dwordx4 v[176:179], v10, s[68:71], s40 offen\n"
      "v_mfma_f32_16x16x32_bf16 a[32:35], a[176:179], v[80:83], a[32:35]\n"
      "ds_read_b128 a[168:171], v230 offset:32\n"
      "v_cndmask_b32_dpp v46, v62, v243, vcc row_ror:8 row_mask:0xf bank_mask:0xf\n"
      "v_mov_b32_dpp v62, v243 row_ror:8 row_mask:0xf bank_mask:0x3\n"
      "v_mfma_f32_16x16x32_bf16 a[36:39], a[176:179], v[84:87], a[36:39]\n"
      "v_perm_b32 v241, v188, v184, s43\n"
      "v_perm_b32 v55, v190, v186, s43\n"
      "v_mfma_f32_16x16x32_bf16 a[40:43], a[176:179], v[88:91], a[40:43]\n"
      "v_cndmask_b32_dpp v35, v51, v240, vcc row_ror:8 row_mask:0xf bank_mask:0xf\n"
      "v_mfma_f32_16x16x32_bf16 a[44:47], a[176:179], v[92:95], a[44:47]\n"
      "v_mov_b32_dpp v51, v240 row_ror:8 row_mask:0xf bank_mask:0x3\n"
      "v_mfma_f32_16x16x32_bf16 a[48:51], a[180:183], v[80:83], a[48:51]\n"
      "ds_read_b128 a[172:175], v231 offset:32\n"
      "v_perm_b32 v242, v189, v185, s42\n"
      "v_perm_b32 v59, v191, v187, s42\n"
      "v_mfma_f32_16x16x32_bf16 a[52:55], a[180:183], v[84:87], a[52:55]\n"
      "buffer_load_dwordx4 v[180:183], v10, s[68:71], s40 offen offset:1024\n"
      "v_mfma_f32_16x16x32_bf16 a[56:59], a[180:183], v[88:91], a[56:59]\n"
      "v_cndmask_b32_dpp v39, v55, v241, vcc row_ror:8 row_mask:0xf bank_mask:0xf\n"
      "v_mov_b32_dpp v55, v241 row_ror:8 row_mask:0xf bank_mask:0x3\n"
      "v_mfma_f32_16x16x32_bf16 a[60:63], a[180:183], v[92:95], a[60:63]\n"
      "v_perm_b32 v243, v189, v185, s43\n"
      "s_waitcnt lgkmcnt(8)\n"
      "v_mfma_f32_16x16x32_bf16 a[64:67], a[128:131], v[80:83], a[64:67]\n"
      "ds_read_b128 a[176:179], v224 offset:48\n"
      "v_perm_b32 v63, v191, v187, s43\n"
      "v_cndmask_b32_dpp v43, v59, v242, vcc row_ror:8 row_mask:0xf bank_mask:0xf\n"
      "v_mfma_f32_16x16x32_bf16 a[68:71], a[128:131], v[84:87], a[68:71]\n"
      "v_mov_b32_dpp v59, v242 row_ror:8 row_mask:0xf bank_mask:0x3\n"
      "v_mfma_f32_16x16x32_bf16 a[72:75], a[128:131], v[88:91], a[72:75]\n"
      "buffer_load_dwordx4 v[184:187], v10, s[68:71], s40 offen offset:2048\n"
      "v_mfma_f32_16x16x32_bf16 a[76:79], a[128:131], v[92:95], a[76:79]\n"
      "v_cndmask_b32_dpp v47, v63, v243, vcc row_ror:8 row_mask:0xf bank_mask:0xf\n"
      "v_mov_b32_dpp v63, v243 row_ror:8 row_mask:0xf bank_mask:0x3\n"
      "v_mfma_f32_16x16x32_bf16 a[80:83], a[132:135], v[80:83], a[80:83]\n"
      "ds_read_b128 a[180:183], v225 offset:48\n"
      "v_mfma_f32_16x16x32_bf16 a[84:87], a[132:135], v[84:87], a[84:87]\n"
      "v_mfma_f32_16x16x32_bf16 a[88:91], a[132:135], v[88:91], a[88:91]\n"
      "v_mfma_f32_16x16x32_bf16 a[92:95], a[132:135], v[92:95], a[92:95]\n"
      "buffer_load_dwordx4 v[188:191], v10, s[68:71], s40 offen offset:3072\n"
      "v_mfma_f32_16x16x32_bf16 a[96:99], a[136:139], v[80:83], a[96:99]\n"
      "ds_read_b128 a[128:131], v226 offset:48\n"
      "s_mov_b32 s40, 10485760\n"
      "v_mfma_f32_16x16x32_bf16 a[100:103], a[136:139], v[84:87], a[100:103]\n"
      "v_mfma_f32_16x16x32_bf16 a[104:107], a[136:139], v[88:91], a[104:107]\n"
      "v_mfma_f32_16x16x32_bf16 a[108:111], a[136:139], v[92:95], a[108:111]\n"
      "v_mfma_f32_16x16x32_bf16 a[112:115], a[140:143], v[80:83], a[112:115]\n"
      "ds_read_b128 a[132:135], v227 offset:48\n"
      "v_mfma_f32_16x16x32_bf16 a[116:119], a[140:143], v[84:87], a[116:119]\n"
      "s_mov_b32 s45, 262144\n"
      "buffer_store_dwordx4 v[244:247], v29, s[76:79], s45 offen sc1\n"
      "v_mfma_f32_16x16x32_bf16 a[120:123], a[140:143], v[88:91], a[120:123]\n"
      "v_mfma_f32_16x16x32_bf16 a[124:127], a[140:143], v[92:95], a[124:127]\n"
      "s_waitcnt lgkmcnt(8)\n"
      "v_mfma_f32_16x16x32_bf16 a[0:3], a[144:147], v[32:35], a[0:3]\n"
      "ds_read_b128 a[136:139], v228 offset:48\n"
      "s_waitcnt vmcnt(34)\n"
      "v_perm_b32 v240, v196, v192, s42\n"
      "v_mfma_f32_16x16x32_bf16 a[4:7], a[144:147], v[36:39], a[4:7]\n"
      "v_perm_b32 v80, v198, v194, s42\n"
      "v_perm_b32 v241, v196, v192, s43\n"
      "v_mfma_f32_16x16x32_bf16 a[8:11], a[144:147], v[40:43], a[8:11]\n"
      "v_perm_b32 v84, v198, v194, s43\n"
      "v_cndmask_b32_dpp v64, v80, v240, vcc row_ror:8 row_mask:0xf bank_mask:0xf\n"
      "v_mfma_f32_16x16x32_bf16 a[12:15], a[144:147], v[44:47], a[12:15]\n"
      "v_mov_b32_dpp v80, v240 row_ror:8 row_mask:0xf bank_mask:0x3\n"
      "v_perm_b32 v242, v197, v193, s42\n"
      "v_mfma_f32_16x16x32_bf16 a[16:19], a[148:151], v[32:35], a[16:19]\n"
      "ds_read_b128 a[140:143], v229 offset:48\n"
      "v_perm_b32 v88, v199, v195, s42\n"
      "v_cndmask_b32_dpp v68, v84, v241, vcc row_ror:8 row_mask:0xf bank_mask:0xf\n"
      "v_mfma_f32_16x16x32_bf16 a[20:23], a[148:151], v[36:39], a[20:23]\n"
      "v_mov_b32_dpp v84, v241 row_ror:8 row_mask:0xf bank_mask:0x3\n"
      "v_perm_b32 v243, v197, v193, s43\n"
      "v_mfma_f32_16x16x32_bf16 a[24:27], a[148:151], v[40:43], a[24:27]\n"
      "v_perm_b32 v92, v199, v195, s43\n"
      "v_cndmask_b32_dpp v72, v88, v242, vcc row_ror:8 row_mask:0xf bank_mask:0xf\n"
      "v_mfma_f32_16x16x32_bf16 a[28:31], a[148:151], v[44:47], a[28:31]\n"
      "buffer_load_dwordx4 v[192:195], v10, s[68:71], s40 offen\n"
      "v_mfma_f32_16x16x32_bf16 a[32:35], a[152:155], v[32:35], a[32:35]\n"
      "ds_read_b128 a[144:147], v230 offset:48\n"
      "v_mov_b32_dpp v88, v242 row_ror:8 row_mask:0xf bank_mask:0x3\n"
      "s_waitcnt vmcnt(33)\n"
      "v_perm_b32 v240, v204, v200, s42\n"
      "v_mfma_f32_16x16x32_bf16 a[36:39], a[152:155], v[36:39], a[36:39]\n"
      "v_perm_b32 v81, v206, v202, s42\n"
      "v_cndmask_b32_dpp v76, v92, v243, vcc row_ror:8 row_mask:0xf bank_mask:0xf\n"
      "v_mfma_f32_16x16x32_bf16 a[40:43], a[152:155], v[40:43], a[40:43]\n"
      "v_mov_b32_dpp v92, v243 row_ror:8 row_mask:0xf bank_mask:0x3\n"
      "v_perm_b32 v241, v204, v200, s43\n"
      "v_mfma_f32_16x16x32_bf16 a[44:47], a[152:155], v[44:47], a[44:47]\n"
      "v_perm_b32 v85, v206, v202, s43\n"
      "v_cndmask_b32_dpp v65, v81, v240, vcc row_ror:8 row_mask:0xf bank_mask:0xf\n"
      "v_mfma_f32_16x16x32_bf16 a[48:51], a[156:159], v[32:35], a[48:51]\n"
      "ds_read_b128 a[148:151], v231 offset:48\n"
      "v_mov_b32_dpp v81, v240 row_ror:8 row_mask:0xf bank_mask:0x3\n"
      "v_perm_b32 v242, v205, v201, s42\n"
      "v_mfma_f32_16x16x32_bf16 a[52:55], a[156:159], v[36:39], a[52:55]\n"
      "buffer_load_dwordx4 v[196:199], v10, s[68:71], s40 offen offset:1024\n"
      "v_mfma_f32_16x16x32_bf16 a[56:59], a[156:159], v[40:43], a[56:59]\n"
      "ds_read_b32 v224, v12 offset:256\n"
      "v_perm_b32 v89, v207, v203, s42\n"
      "v_cndmask_b32_dpp v69, v85, v241, vcc row_ror:8 row_mask:0xf bank_mask:0xf\n"
      "v_mfma_f32_16x16x32_bf16 a[60:63], a[156:159], v[44:47], a[60:63]\n"
      "v_mov_b32_dpp v85, v241 row_ror:8 row_mask:0xf bank_mask:0x3\n"
      "v_perm_b32 v243, v205, v201, s43\n"
      "s_waitcnt lgkmcnt(9)\n"
      "v_mfma_f32_16x16x32_bf16 a[64:67], a[160:163], v[32:35], a[64:67]\n"
      "ds_read_b128 a[152:155], v232\n"
      "v_perm_b32 v93, v207, v203, s43\n"
      "v_cndmask_b32_dpp v73, v89, v242, vcc row_ror:8 row_mask:0xf bank_mask:0xf\n"
      "v_mfma_f32_16x16x32_bf16 a[68:71], a[160:163], v[36:39], a[68:71]\n"
      "ds_read_b32 v225, v13 offset:256\n"
      "v_mov_b32_dpp v89, v242 row_ror:8 row_mask:0xf bank_mask:0x3\n"
      "s_waitcnt vmcnt(32)\n"
      "v_perm_b32 v240, v212, v208, s42\n"
      "v_mfma_f32_16x16x32_bf16 a[72:75], a[160:163], v[40:43], a[72:75]\n"
      "buffer_load_dwordx4 v[200:203], v10, s[68:71], s40 offen offset:2048\n"
      "v_mfma_f32_16x16x32_bf16 a[76:79], a[160:163], v[44:47], a[76:79]\n"
      "v_perm_b32 v82, v214, v210, s42\n"
      "v_cndmask_b32_dpp v77, v93, v243, vcc row_ror:8 row_mask:0xf bank_mask:0xf\n"
      "v_mfma_f32_16x16x32_bf16 a[80:83], a[164:167], v[32:35], a[80:83]\n"
      "ds_read_b128 a[156:159], v233\n"
      "v_mov_b32_dpp v93, v243 row_ror:8 row_mask:0xf bank_mask:0x3\n"
      "v_perm_b32 v241, v212, v208, s43\n"
      "v_mfma_f32_16x16x32_bf16 a[84:87], a[164:167], v[36:39], a[84:87]\n"
      "ds_read_b32 v226, v14 offset:256\n"
      "v_perm_b32 v86, v214, v210, s43\n"
      "v_cndmask_b32_dpp v66, v82, v240, vcc row_ror:8 row_mask:0xf bank_mask:0xf\n"
      "v_mfma_f32_16x16x32_bf16 a[88:91], a[164:167], v[40:43], a[88:91]\n"
      "ds_read_b32 v227, v15 offset:256\n"
      "v_mov_b32_dpp v82, v240 row_ror:8 row_mask:0xf bank_mask:0x3\n"
      "v_perm_b32 v242, v213, v209, s42\n"
      "v_mfma_f32_16x16x32_bf16 a[92:95], a[164:167], v[44:47], a[92:95]\n"
      "buffer_load_dwordx4 v[204:207], v10, s[68:71], s40 offen offset:3072\n"
      "v_mfma_f32_16x16x32_bf16 a[96:99], a[168:171], v[32:35], a[96:99]\n"
      "ds_read_b128 a[160:163], v234\n"
      "s_mov_b32 s40, 10489856\n"
      "v_perm_b32 v90, v215, v211, s42\n"
      "v_mfma_f32_16x16x32_bf16 a[100:103], a[168:171], v[36:39], a[100:103]\n"
      "ds_read_b32 v228, v12 offset:25856\n"
      "v_cndmask_b32_dpp v70, v86, v241, vcc row_ror:8 row_mask:0xf bank_mask:0xf\n"
      "v_mov_b32_dpp v86, v241 row_ror:8 row_mask:0xf bank_mask:0x3\n"
      "v_mfma_f32_16x16x32_bf16 a[104:107], a[168:171], v[40:43], a[104:107]\n"
      "ds_read_b32 v229, v13 offset:25856\n"
      "v_perm_b32 v243, v213, v209, s43\n"
      "v_perm_b32 v94, v215, v211, s43\n"
      "v_mfma_f32_16x16x32_bf16 a[108:111], a[168:171], v[44:47], a[108:111]\n"
      "v_cndmask_b32_dpp v74, v90, v242, vcc row_ror:8 row_mask:0xf bank_mask:0xf\n"
      "v_mov_b32_dpp v90, v242 row_ror:8 row_mask:0xf bank_mask:0x3\n"
      "v_mfma_f32_16x16x32_bf16 a[112:115], a[172:175], v[32:35], a[112:115]\n"
      "ds_read_b128 a[164:167], v235\n"
      "s_waitcnt vmcnt(32)\n"
      "v_perm_b32 v240, v220, v216, s42\n"
      "v_mfma_f32_16x16x32_bf16 a[116:119], a[172:175], v[36:39], a[116:119]\n"
      "buffer_load_dwordx4 v[208:211], v10, s[68:71], s40 offen\n"
      "v_mfma_f32_16x16x32_bf16 a[120:123], a[172:175], v[40:43], a[120:123]\n"
      "ds_read_b32 v230, v14 offset:25856\n"
      "v_perm_b32 v83, v222, v218, s42\n"
      "v_cndmask_b32_dpp v78, v94, v243, vcc row_ror:8 row_mask:0xf bank_mask:0xf\n"
      "v_mfma_f32_16x16x32_bf16 a[124:127], a[172:175], v[44:47], a[124:127]\n"
      "v_mov_b32_dpp v94, v243 row_ror:8 row_mask:0xf bank_mask:0x3\n"
      "v_perm_b32 v241, v220, v216, s43\n"
      "s_waitcnt lgkmcnt(15)\n"
      "v_mfma_f32_16x16x32_bf16 a[0:3], a[176:179], v[48:51], a[0:3]\n"
      "ds_read_b128 a[168:171], v236\n"
      "v_perm_b32 v87, v222, v218, s43\n"
      "v_cndmask_b32_dpp v67, v83, v240, vcc row_ror:8 row_mask:0xf bank_mask:0xf\n"
      "v_mfma_f32_16x16x32_bf16 a[4:7], a[176:179], v[52:55], a[4:7]\n"
      "ds_read_b32 v231, v15 offset:25856\n"
      "v_mov_b32_dpp v83, v240 row_ror:8 row_mask:0xf bank_mask:0x3\n"
      "v_perm_b32 v242, v221, v217, s42\n"
      "v_mfma_f32_16x16x32_bf16 a[8:11], a[176:179], v[56:59], a[8:11]\n"
      "buffer_load_dwordx4 v[212:215], v10, s[68:71], s40 offen offset:1024\n"
      "v_mfma_f32_16x16x32_bf16 a[12:15], a[176:179], v[60:63], a[12:15]\n"
      "v_perm_b32 v91, v223, v219, s42\n"
      "v_cndmask_b32_dpp v71, v87, v241, vcc row_ror:8 row_mask:0xf bank_mask:0xf\n"
      "v_mfma_f32_16x16x32_bf16 a[16:19], a[180:183], v[48:51], a[16:19]\n"
      "ds_read_b128 a[172:175], v237\n"
      "v_mov_b32_dpp v87, v241 row_ror:8 row_mask:0xf bank_mask:0x3\n"
      "v_perm_b32 v243, v221, v217, s43\n"
      "v_mfma_f32_16x16x32_bf16 a[20:23], a[180:183], v[52:55], a[20:23]\n"
      "s_mov_b32 s45, 270336\n"
      "buffer_store_dwordx4 v[244:247], v29, s[76:79], s45 offen sc1\n"
      "v_mfma_f32_16x16x32_bf16 a[24:27], a[180:183], v[56:59], a[24:27]\n"
      "v_perm_b32 v95, v223, v219, s43\n"
      "v_cndmask_b32_dpp v75, v91, v242, vcc row_ror:8 row_mask:0xf bank_mask:0xf\n"
      "v_mfma_f32_16x16x32_bf16 a[28:31], a[180:183], v[60:63], a[28:31]\n"
      "buffer_load_dwordx4 v[216:219], v10, s[68:71], s40 offen offset:2048\n"
      "v_mfma_f32_16x16x32_bf16 a[32:35], a[128:131], v[48:51], a[32:35]\n"
      "ds_read_b128 a[176:179], v238\n"
      "v_mov_b32_dpp v91, v242 row_ror:8 row_mask:0xf bank_mask:0x3\n"
      "v_cndmask_b32_dpp v79, v95, v243, vcc row_ror:8 row_mask:0xf bank_mask:0xf\n"
      "v_mfma_f32_16x16x32_bf16 a[36:39], a[128:131], v[52:55], a[36:39]\n"
      "v_mov_b32_dpp v95, v243 row_ror:8 row_mask:0xf bank_mask:0x3\n"
      "v_mfma_f32_16x16x32_bf16 a[40:43], a[128:131], v[56:59], a[40:43]\n"
      "v_mfma_f32_16x16x32_bf16 a[44:47], a[128:131], v[60:63], a[44:47]\n"
      "v_mfma_f32_16x16x32_bf16 a[48:51], a[132:135], v[48:51], a[48:51]\n"
      "ds_read_b128 a[180:183], v239\n"
      "v_mfma_f32_16x16x32_bf16 a[52:55], a[132:135], v[52:55], a[52:55]\n"
      "buffer_load_dwordx4 v[220:223], v10, s[68:71], s40 offen offset:3072\n"
      "v_mfma_f32_16x16x32_bf16 a[56:59], a[132:135], v[56:59], a[56:59]\n"
      "s_mov_b32 s40, 10502144\n"
      "v_mfma_f32_16x16x32_bf16 a[60:63], a[132:135], v[60:63], a[60:63]\n"
      "s_waitcnt lgkmcnt(15)\n"
      "v_mfma_f32_16x16x32_bf16 a[64:67], a[136:139], v[48:51], a[64:67]\n"
      "ds_read_b128 a[128:131], v232 offset:16\n"
      "v_mfma_f32_16x16x32_bf16 a[68:71], a[136:139], v[52:55], a[68:71]\n"
      "v_mfma_f32_16x16x32_bf16 a[72:75], a[136:139], v[56:59], a[72:75]\n"
      "v_mfma_f32_16x16x32_bf16 a[76:79], a[136:139], v[60:63], a[76:79]\n"
      "v_mfma_f32_16x16x32_bf16 a[80:83], a[140:143], v[48:51], a[80:83]\n"
      "ds_read_b128 a[132:135], v233 offset:16\n"
      "v_mfma_f32_16x16x32_bf16 a[84:87], a[140:143], v[52:55], a[84:87]\n"
      "v_mul_u32_u24 v224, s46, v224\n"
      "v_mfma_f32_16x16x32_bf16 a[88:91], a[140:143], v[56:59], a[88:91]\n"
      "s_waitcnt lgkmcnt(15)\n"
      "v_mul_u32_u24 v225, s46, v225\n"
      "v_mfma_f32_16x16x32_bf16 a[92:95], a[140:143], v[60:63], a[92:95]\n"
      "v_mfma_f32_16x16x32_bf16 a[96:99], a[144:147], v[48:51], a[96:99]\n"
      "ds_read_b128 a[136:139], v234 offset:16\n"
      "v_mfma_f32_16x16x32_bf16 a[100:103], a[144:147], v[52:55], a[100:103]\n"
      "s_waitcnt lgkmcnt(14)\n"
      "v_mul_u32_u24 v226, s46, v226\n"
      "v_mfma_f32_16x16x32_bf16 a[104:107], a[144:147], v[56:59], a[104:107]\n"
      "s_waitcnt lgkmcnt(13)\n"
      "v_mul_u32_u24 v227, s46, v227\n"
      "v_mfma_f32_16x16x32_bf16 a[108:111], a[144:147], v[60:63], a[108:111]\n"
      "v_mfma_f32_16x16x32_bf16 a[112:115], a[148:151], v[48:51], a[112:115]\n"
      "ds_read_b128 a[140:143], v235 offset:16\n"
      "v_mfma_f32_16x16x32_bf16 a[116:119], a[148:151], v[52:55], a[116:119]\n"
      "s_waitcnt lgkmcnt(12)\n"
      "v_mul_u32_u24 v228, s46, v228\n"
      "v_mfma_f32_16x16x32_bf16 a[120:123], a[148:151], v[56:59], a[120:123]\n"
      "s_waitcnt lgkmcnt(11)\n"
      "v_mul_u32_u24 v229, s46, v229\n"
      "v_mfma_f32_16x16x32_bf16 a[124:127], a[148:151], v[60:63], a[124:127]\n"
      "v_mfma_f32_16x16x32_bf16 a[0:3], a[152:155], v[64:67], a[0:3]\n"
      "ds_read_b128 a[144:147], v236 offset:16\n"
      "s_waitcnt vmcnt(35)\n"
      "v_perm_b32 v240, v100, v96, s42\n"
      "v_mfma_f32_16x16x32_bf16 a[4:7], a[152:155], v[68:71], a[4:7]\n"
      "s_waitcnt lgkmcnt(10)\n"
      "v_mul_u32_u24 v230, s46, v230\n"
      "v_perm_b32 v48, v102, v98, s42\n"
      "v_mfma_f32_16x16x32_bf16 a[8:11], a[152:155], v[72:75], a[8:11]\n"
      "s_waitcnt lgkmcnt(8)\n"
      "v_mul_u32_u24 v231, s46, v231\n"
      "v_perm_b32 v241, v100, v96, s43\n"
      "v_mfma_f32_16x16x32_bf16 a[12:15], a[152:155], v[76:79], a[12:15]\n"
      "v_perm_b32 v52, v102, v98, s43\n"
      "v_cndmask_b32_dpp v32, v48, v240, vcc row_ror:8 row_mask:0xf bank_mask:0xf\n"
      "v_mfma_f32_16x16x32_bf16 a[16:19], a[156:159], v[64:67], a[16:19]\n"
      "ds_read_b128 a[148:151], v237 offset:16\n"
      "v_mov_b32_dpp v48, v240 row_ror:8 row_mask:0xf bank_mask:0x3\n"
      "v_perm_b32 v242, v101, v97, s42\n"
      "v_mfma_f32_16x16x32_bf16 a[20:23], a[156:159], v[68:71], a[20:23]\n"
      "v_and_b32 v224, -16, v224\n"
      "v_perm_b32 v56, v103, v99, s42\n"
      "v_mfma_f32_16x16x32_bf16 a[24:27], a[156:159], v[72:75], a[24:27]\n"
      "v_and_b32 v225, -16, v225\n"
      "v_cndmask_b32_dpp v36, v52, v241, vcc row_ror:8 row_mask:0xf bank_mask:0xf\n"
      "v_mfma_f32_16x16x32_bf16 a[28:31], a[156:159], v[76:79], a[28:31]\n"
      "v_mov_b32_dpp v52, v241 row_ror:8 row_mask:0xf bank_mask:0x3\n"
      "v_perm_b32 v243, v101, v97, s43\n"
      "v_mfma_f32_16x16x32_bf16 a[32:35], a[160:163], v[64:67], a[32:35]\n"
      "ds_read_b128 a[152:155], v238 offset:16\n"
      "v_perm_b32 v60, v103, v99, s43\n"
      "v_cndmask_b32_dpp v40, v56, v242, vcc row_ror:8 row_mask:0xf bank_mask:0xf\n"
      "v_mfma_f32_16x16x32_bf16 a[36:39], a[160:163], v[68:71], a[36:39]\n"
      "v_and_b32 v226, -16, v226\n"
      "v_mov_b32_dpp v56, v242 row_ror:8 row_mask:0xf bank_mask:0x3\n"
      "v_mfma_f32_16x16x32_bf16 a[40:43], a[160:163], v[72:75], a[40:43]\n"
      "v_and_b32 v227, -16, v227\n"
      "s_waitcnt vmcnt(33)\n"
      "v_perm_b32 v240, v108, v104, s42\n"
      "v_mfma_f32_16x16x32_bf16 a[44:47], a[160:163], v[76:79], a[44:47]\n"
      "v_perm_b32 v49, v110, v106, s42\n"
      "v_cndmask_b32_dpp v44, v60, v243, vcc row_ror:8 row_mask:0xf bank_mask:0xf\n"
      "v_mfma_f32_16x16x32_bf16 a[48:51], a[164:167], v[64:67], a[48:51]\n"
      "ds_read_b128 a[156:159], v239 offset:16\n"
      "v_mov_b32_dpp v60, v243 row_ror:8 row_mask:0xf bank_mask:0x3\n"
      "v_perm_b32 v241, v108, v104, s43\n"
      "v_mfma_f32_16x16x32_bf16 a[52:55], a[164:167], v[68:71], a[52:55]\n"
      "v_and_b32 v228, -16, v228\n"
      "v_perm_b32 v53, v110, v106, s43\n"
      "v_mfma_f32_16x16x32_bf16 a[56:59], a[164:167], v[72:75], a[56:59]\n"
      "v_and_b32 v229, -16, v229\n"
      "v_cndmask_b32_dpp v33, v49, v240, vcc row_ror:8 row_mask:0xf bank_mask:0xf\n"
      "v_mfma_f32_16x16x32_bf16 a[60:63], a[164:167], v[76:79], a[60:63]\n"
      "v_mov_b32_dpp v49, v240 row_ror:8 row_mask:0xf bank_mask:0x3\n"
      "v_perm_b32 v242, v109, v105, s42\n"
      "v_mfma_f32_16x16x32_bf16 a[64:67], a[168:171], v[64:67], a[64:67]\n"
      "ds_read_b128 a[160:163], v232 offset:32\n"
      "v_perm_b32 v57, v111, v107, s42\n"
      "v_cndmask_b32_dpp v37, v53, v241, vcc row_ror:8 row_mask:0xf bank_mask:0xf\n"
      "v_mfma_f32_16x16x32_bf16 a[68:71], a[168:171], v[68:71], a[68:71]\n"
      "v_and_b32 v230, -16, v230\n"
      "v_mov_b32_dpp v53, v241 row_ror:8 row_mask:0xf bank_mask:0x3\n"
      "v_mfma_f32_16x16x32_bf16 a[72:75], a[168:171], v[72:75], a[72:75]\n"
      "v_and_b32 v231, -16, v231\n"
      "v_perm_b32 v243, v109, v105, s43\n"
      "v_mfma_f32_16x16x32_bf16 a[76:79], a[168:171], v[76:79], a[76:79]\n"
      "v_perm_b32 v61, v111, v107, s43\n"
      "v_cndmask_b32_dpp v41, v57, v242, vcc row_ror:8 row_mask:0xf bank_mask:0xf\n"
      "s_waitcnt lgkmcnt(8)\n"
      "v_mfma_f32_16x16x32_bf16 a[80:83], a[172:175], v[64:67], a[80:83]\n"
      "ds_read_b128 a[164:167], v233 offset:32\n"
      "v_mov_b32_dpp v57, v242 row_ror:8 row_mask:0xf bank_mask:0x3\n"
      "s_waitcnt vmcnt(31)\n"
      "v_perm_b32 v240, v116, v112, s42\n"
      "v_mfma_f32_16x16x32_bf16 a[84:87], a[172:175], v[68:71], a[84:87]\n"
      "v_perm_b32 v50, v118, v114, s42\n"
      "v_cndmask_b32_dpp v45, v61, v243, vcc row_ror:8 row_mask:0xf bank_mask:0xf\n"
      "v_mfma_f32_16x16x32_bf16 a[88:91], a[172:175], v[72:75], a[88:91]\n"
      "v_mov_b32_dpp v61, v243 row_ror:8 row_mask:0xf bank_mask:0x3\n"
      "v_perm_b32 v241, v116, v112, s43\n"
      "v_mfma_f32_16x16x32_bf16 a[92:95], a[172:175], v[76:79], a[92:95]\n"
      "v_perm_b32 v54, v118, v114, s43\n"
      "v_cndmask_b32_dpp v34, v50, v240, vcc row_ror:8 row_mask:0xf bank_mask:0xf\n"
      "v_mfma_f32_16x16x32_bf16 a[96:99], a[176:179], v[64:67], a[96:99]\n"
      "ds_read_b128 a[168:171], v234 offset:32\n"
      "v_mov_b32_dpp v50, v240 row_ror:8 row_mask:0xf bank_mask:0x3\n"
      "v_perm_b32 v242, v117, v113, s42\n"
      "v_mfma_f32_16x16x32_bf16 a[100:103], a[176:179], v[68:71], a[100:103]\n"
      "v_perm_b32 v58, v119, v115, s42\n"
      "v_cndmask_b32_dpp v38, v54, v241, vcc row_ror:8 row_mask:0xf bank_mask:0xf\n"
      "v_mfma_f32_16x16x32_bf16 a[104:107], a[176:179], v[72:75], a[104:107]\n"
      "v_mov_b32_dpp v54, v241 row_ror:8 row_mask:0xf bank_mask:0x3\n"
      "v_perm_b32 v243, v117, v113, s43\n"
      "v_mfma_f32_16x16x32_bf16 a[108:111], a[176:179], v[76:79], a[108:111]\n"
      "v_perm_b32 v62, v119, v115, s43\n"
      "v_cndmask_b32_dpp v42, v58, v242, vcc row_ror:8 row_mask:0xf bank_mask:0xf\n"
      "v_mfma_f32_16x16x32_bf16 a[112:115], a[180:183], v[64:67], a[112:115]\n"
      "ds_read_b128 a[172:175], v235 offset:32\n"
      "v_mov_b32_dpp v58, v242 row_ror:8 row_mask:0xf bank_mask:0x3\n"
      "s_waitcnt vmcnt(29)\n"
      "v_perm_b32 v240, v124, v120, s42\n"
      "v_mfma_f32_16x16x32_bf16 a[116:119], a[180:183], v[68:71], a[116:119]\n"
      "v_perm_b32 v51, v126, v122, s42\n"
      "v_cndmask_b32_dpp v46, v62, v243, vcc row_ror:8 row_mask:0xf bank_mask:0xf\n"
      "v_mfma_f32_16x16x32_bf16 a[120:123], a[180:183], v[72:75], a[120:123]\n"
      "v_mov_b32_dpp v62, v243 row_ror:8 row_mask:0xf bank_mask:0x3\n"
      "v_perm_b32 v241, v124, v120, s43\n"
      "v_mfma_f32_16x16x32_bf16 a[124:127], a[180:183], v[76:79], a[124:127]\n"
      "v_perm_b32 v55, v126, v122, s43\n"
      "v_cndmask_b32_dpp v35, v51, v240, vcc row_ror:8 row_mask:0xf bank_mask:0xf\n"
      "v_mfma_f32_16x16x32_bf16 a[0:3], a[128:131], v[80:83], a[0:3]\n"
      "ds_read_b128 a[176:179], v236 offset:32\n"
      "v_mov_b32_dpp v51, v240 row_ror:8 row_mask:0xf bank_mask:0x3\n"
      "v_perm_b32 v242, v125, v121, s42\n"
      "v_mfma_f32_16x16x32_bf16 a[4:7], a[128:131], v[84:87], a[4:7]\n"
      "v_perm_b32 v59, v127, v123, s42\n"
      "v_cndmask_b32_dpp v39, v55, v241, vcc row_ror:8 row_mask:0xf bank_mask:0xf\n"
      "v_mfma_f32_16x16x32_bf16 a[8:11], a[128:131], v[88:91], a[8:11]\n"
      "v_mov_b32_dpp v55, v241 row_ror:8 row_mask:0xf bank_mask:0x3\n"
      "v_perm_b32 v243, v125, v121, s43\n"
      "v_mfma_f32_16x16x32_bf16 a[12:15], a[128:131], v[92:95], a[12:15]\n"
      "v_perm_b32 v63, v127, v123, s43\n"
      "v_cndmask_b32_dpp v43, v59, v242, vcc row_ror:8 row_mask:0xf bank_mask:0xf\n"
      "s_waitcnt lgkmcnt(8)\n"
      "v_mfma_f32_16x16x32_bf16 a[16:19], a[132:135], v[80:83], a[16:19]\n"
      "ds_read_b128 a[180:183], v237 offset:32\n"
      "v_mov_b32_dpp v59, v242 row_ror:8 row_mask:0xf bank_mask:0x3\n"
      "v_cndmask_b32_dpp v47, v63, v243, vcc row_ror:8 row_mask:0xf bank_mask:0xf\n"
      "v_mfma_f32_16x16x32_bf16 a[20:23], a[132:135], v[84:87], a[20:23]\n"
      "v_mov_b32_dpp v63, v243 row_ror:8 row_mask:0xf bank_mask:0x3\n"
      "s_waitcnt vmcnt(27)\n"
      "v_perm_b32 v240, v132, v128, s42\n"
      "v_mfma_f32_16x16x32_bf16 a[24:27], a[132:135], v[88:91], a[24:27]\n"
      "v_perm_b32 v112, v134, v130, s42\n"
      "v_perm_b32 v241, v132, v128, s43\n"
      "v_mfma_f32_16x16x32_bf16 a[28:31], a[132:135], v[92:95], a[28:31]\n"
      "v_perm_b32 v116, v134, v130, s43\n"
      "v_cndmask_b32_dpp v96, v112, v240, vcc row_ror:8 row_mask:0xf bank_mask:0xf\n"
      "v_mfma_f32_16x16x32_bf16 a[32:35], a[136:139], v[80:83], a[32:35]\n"
      "ds_read_b128 a[128:131], v238 offset:32\n"
      "v_mov_b32_dpp v112, v240 row_ror:8 row_mask:0xf bank_mask:0x3\n"
      "v_perm_b32 v242, v133, v129, s42\n"
      "v_mfma_f32_16x16x32_bf16 a[36:39], a[136:139], v[84:87], a[36:39]\n"
      "v_perm_b32 v120, v135, v131, s42\n"
      "v_cndmask_b32_dpp v100, v116, v241, vcc row_ror:8 row_mask:0xf bank_mask:0xf\n"
      "v_mfma_f32_16x16x32_bf16 a[40:43], a[136:139], v[88:91], a[40:43]\n"
      "v_mov_b32_dpp v116, v241 row_ror:8 row_mask:0xf bank_mask:0x3\n"
      "v_perm_b32 v243, v133, v129, s43\n"
      "v_mfma_f32_16x16x32_bf16 a[44:47], a[136:139], v[92:95], a[44:47]\n"
      "v_perm_b32 v124, v135, v131, s43\n"
      "v_cndmask_b32_dpp v104, v120, v242, vcc row_ror:8 row_mask:0xf bank_mask:0xf\n"
      "v_mfma_f32_16x16x32_bf16 a[48:51], a[140:143], v[80:83], a[48:51]\n"
      "ds_read_b128 a[132:135], v239 offset:32\n"
      "v_mov_b32_dpp v120, v242 row_ror:8 row_mask:0xf bank_mask:0x3\n"
      "s_waitcnt vmcnt(24)\n"
      "v_perm_b32 v240, v140, v136, s42\n"
      "v_mfma_f32_16x16x32_bf16 a[52:55], a[140:143], v[84:87], a[52:55]\n"
      "buffer_load_dwordx4 v[128:131], v10, s[68:71], s40 offen\n"
      "v_mfma_f32_16x16x32_bf16 a[56:59], a[140:143], v[88:91], a[56:59]\n"
      "v_perm_b32 v113, v142, v138, s42\n"
      "v_cndmask_b32_dpp v108, v124, v243, vcc row_ror:8 row_mask:0xf bank_mask:0xf\n"
      "v_mfma_f32_16x16x32_bf16 a[60:63], a[140:143], v[92:95], a[60:63]\n"
      "v_mov_b32_dpp v124, v243 row_ror:8 row_mask:0xf bank_mask:0x3\n"
      "v_perm_b32 v241, v140, v136, s43\n"
      "v_mfma_f32_16x16x32_bf16 a[64:67], a[144:147], v[80:83], a[64:67]\n"
      "ds_read_b128 a[136:139], v232 offset:48\n"
      "v_perm_b32 v117, v142, v138, s43\n"
      "v_cndmask_b32_dpp v97, v113, v240, vcc row_ror:8 row_mask:0xf bank_mask:0xf\n"
      "v_mfma_f32_16x16x32_bf16 a[68:71], a[144:147], v[84:87], a[68:71]\n"
      "v_mov_b32_dpp v113, v240 row_ror:8 row_mask:0xf bank_mask:0x3\n"
      "v_perm_b32 v242, v141, v137, s42\n"
      "v_mfma_f32_16x16x32_bf16 a[72:75], a[144:147], v[88:91], a[72:75]\n"
      "buffer_load_dwordx4 v[132:135], v10, s[68:71], s40 offen offset:1024\n"
      "v_mfma_f32_16x16x32_bf16 a[76:79], a[144:147], v[92:95], a[76:79]\n"
      "v_perm_b32 v121, v143, v139, s42\n"
      "v_cndmask_b32_dpp v101, v117, v241, vcc row_ror:8 row_mask:0xf bank_mask:0xf\n"
      "s_waitcnt lgkmcnt(8)\n"
      "v_mfma_f32_16x16x32_bf16 a[80:83], a[148:151], v[80:83], a[80:83]\n"
      "ds_read_b128 a[140:143], v233 offset:48\n"
      "v_mov_b32_dpp v117, v241 row_ror:8 row_mask:0xf bank_mask:0x3\n"
      "v_perm_b32 v243, v141, v137, s43\n"
      "v_mfma_f32_16x16x32_bf16 a[84:87], a[148:151], v[84:87], a[84:87]\n"
      "v_perm_b32 v125, v143, v139, s43\n"
      "v_cndmask_b32_dpp v105, v121, v242, vcc row_ror:8 row_mask:0xf bank_mask:0xf\n"
      "v_mfma_f32_16x16x32_bf16 a[88:91], a[148:151], v[88:91], a[88:91]\n"
      "v_mov_b32_dpp v121, v242 row_ror:8 row_mask:0xf bank_mask:0x3\n"
      "s_waitcnt vmcnt(24)\n"
      "v_perm_b32 v240, v148, v144, s42\n"
      "v_mfma_f32_16x16x32_bf16 a[92:95], a[148:151], v[92:95], a[92:95]\n"
      "buffer_load_dwordx4 v[136:139], v10, s[68:71], s40 offen offset:2048\n"
      "v_mfma_f32_16x16x32_bf16 a[96:99], a[152:155], v[80:83], a[96:99]\n"
      "ds_read_b128 a[144:147], v234 offset:48\n"
      "v_perm_b32 v114, v150, v146, s42\n"
      "v_cndmask_b32_dpp v109, v125, v243, vcc row_ror:8 row_mask:0xf bank_mask:0xf\n"
      "v_mfma_f32_16x16x32_bf16 a[100:103], a[152:155], v[84:87], a[100:103]\n"
      "v_mov_b32_dpp v125, v243 row_ror:8 row_mask:0xf bank_mask:0x3\n"
      "v_perm_b32 v241, v148, v144, s43\n"
      "v_mfma_f32_16x16x32_bf16 a[104:107], a[152:155], v[88:91], a[104:107]\n"
      "v_perm_b32 v118, v150, v146, s43\n"
      "v_cndmask_b32_dpp v98, v114, v240, vcc row_ror:8 row_mask:0xf bank_mask:0xf\n"
      "v_mfma_f32_16x16x32_bf16 a[108:111], a[152:155], v[92:95], a[108:111]\n"
      "v_mov_b32_dpp v114, v240 row_ror:8 row_mask:0xf bank_mask:0x3\n"
      "v_perm_b32 v242, v149, v145, s42\n"
      "v_mfma_f32_16x16x32_bf16 a[112:115], a[156:159], v[80:83], a[112:115]\n"
      "ds_read_b128 a[148:151], v235 offset:48\n"
      "v_perm_b32 v122, v151, v147, s42\n"
      "v_cndmask_b32_dpp v102, v118, v241, vcc row_ror:8 row_mask:0xf bank_mask:0xf\n"
      "v_mfma_f32_16x16x32_bf16 a[116:119], a[156:159], v[84:87], a[116:119]\n"
      "buffer_load_dwordx4 v[140:143], v10, s[68:71], s40 offen offset:3072\n"
      "v_mfma_f32_16x16x32_bf16 a[120:123], a[156:159], v[88:91], a[120:123]\n"
      "s_mov_b32 s40, 10506240\n"
      "v_mov_b32_dpp v118, v241 row_ror:8 row_mask:0xf bank_mask:0x3\n"
      "v_perm_b32 v243, v149, v145, s43\n"
      "v_mfma_f32_16x16x32_bf16 a[124:127], a[156:159], v[92:95], a[124:127]\n"
      "v_perm_b32 v126, v151, v147, s43\n"
      "v_cndmask_b32_dpp v106, v122, v242, vcc row_ror:8 row_mask:0xf bank_mask:0xf\n"
      "v_mfma_f32_16x16x32_bf16 a[0:3], a[160:163], v[32:35], a[0:3]\n"
      "ds_read_b128 a[152:155], v236 offset:48\n"
      "v_mov_b32_dpp v122, v242 row_ror:8 row_mask:0xf bank_mask:0x3\n"
      "s_waitcnt vmcnt(24)\n"
      "v_perm_b32 v240, v156, v152, s42\n"
      "v_mfma_f32_16x16x32_bf16 a[4:7], a[160:163], v[36:39], a[4:7]\n"
      "v_perm_b32 v115, v158, v154, s42\n"
      "v_cndmask_b32_dpp v110, v126, v243, vcc row_ror:8 row_mask:0xf bank_mask:0xf\n"
      "v_mfma_f32_16x16x32_bf16 a[8:11], a[160:163], v[40:43], a[8:11]\n"
      "buffer_load_dwordx4 v[144:147], v10, s[68:71], s40 offen\n"
      "v_mfma_f32_16x16x32_bf16 a[12:15], a[160:163], v[44:47], a[12:15]\n"
      "v_mov_b32_dpp v126, v243 row_ror:8 row_mask:0xf bank_mask:0x3\n"
      "v_perm_b32 v241, v156, v152, s43\n"
      "s_waitcnt lgkmcnt(8)\n"
      "v_mfma_f32_16x16x32_bf16 a[16:19], a[164:167], v[32:35], a[16:19]\n"
      "ds_read_b128 a[156:159], v237 offset:48\n"
      "v_perm_b32 v119, v158, v154, s43\n"
      "v_cndmask_b32_dpp v99, v115, v240, vcc row_ror:8 row_mask:0xf bank_mask:0xf\n"
      "v_mfma_f32_16x16x32_bf16 a[20:23], a[164:167], v[36:39], a[20:23]\n"
      "v_mov_b32_dpp v115, v240 row_ror:8 row_mask:0xf bank_mask:0x3\n"
      "v_perm_b32 v242, v157, v153, s42\n"
      "v_mfma_f32_16x16x32_bf16 a[24:27], a[164:167], v[40:43], a[24:27]\n"
      "v_perm_b32 v123, v159, v155, s42\n"
      "v_cndmask_b32_dpp v103, v119, v241, vcc row_ror:8 row_mask:0xf bank_mask:0xf\n"
      "v_mfma_f32_16x16x32_bf16 a[28:31], a[164:167], v[44:47], a[28:31]\n"
      "buffer_load_dwordx4 v[148:151], v10, s[68:71], s40 offen offset:1024\n"
      "v_mfma_f32_16x16x32_bf16 a[32:35], a[168:171], v[32:35], a[32:35]\n"
      "ds_read_b128 a[160:163], v238 offset:48\n"
      "v_mov_b32_dpp v119, v241 row_ror:8 row_mask:0xf bank_mask:0x3\n"
      "v_perm_b32 v243, v157, v153, s43\n"
      "v_mfma_f32_16x16x32_bf16 a[36:39], a[168:171], v[36:39], a[36:39]\n"
      "v_perm_b32 v127, v159, v155, s43\n"
      "v_cndmask_b32_dpp v107, v123, v242, vcc row_ror:8 row_mask:0xf bank_mask:0xf\n"
      "v_mfma_f32_16x16x32_bf16 a[40:43], a[168:171], v[40:43], a[40:43]\n"
      "v_mov_b32_dpp v123, v242 row_ror:8 row_mask:0xf bank_mask:0x3\n"
      "v_cndmask_b32_dpp v111, v127, v243, vcc row_ror:8 row_mask:0xf bank_mask:0xf\n"
      "v_mfma_f32_16x16x32_bf16 a[44:47], a[168:171], v[44:47], a[44:47]\n"
      "v_mov_b32_dpp v127, v243 row_ror:8 row_mask:0xf bank_mask:0x3\n"
      "s_waitcnt vmcnt(22)\n"
      "v_perm_b32 v240, v164, v160, s42\n"
      "v_mfma_f32_16x16x32_bf16 a[48:51], a[172:175], v[32:35], a[48:51]\n"
      "ds_read_b128 a[164:167], v239 offset:48\n"
      "v_perm_b32 v80, v166, v162, s42\n"
      "v_perm_b32 v241, v164, v160, s43\n"
      "v_mfma_f32_16x16x32_bf16 a[52:55], a[172:175], v[36:39], a[52:55]\n"
      "buffer_load_dwordx4 v[152:155], v10, s[68:71], s40 offen offset:2048\n"
      "v_mfma_f32_16x16x32_bf16 a[56:59], a[172:175], v[40:43], a[56:59]\n"
      "ds_read_b32 v232, v12 offset:320\n"
      "v_perm_b32 v84, v166, v162, s43\n"
      "v_cndmask_b32_dpp v64, v80, v240, vcc row_ror:8 row_mask:0xf bank_mask:0xf\n"
      "v_mfma_f32_16x16x32_bf16 a[60:63], a[172:175], v[44:47], a[60:63]\n"
      "v_mov_b32_dpp v80, v240 row_ror:8 row_mask:0xf bank_mask:0x3\n"
      "v_perm_b32 v242, v165, v161, s42\n"
      "v_mfma_f32_16x16x32_bf16 a[64:67], a[176:179], v[32:35], a[64:67]\n"
      "ds_read_b128 a[168:171], v224\n"
      "v_perm_b32 v88, v167, v163, s42\n"
      "v_cndmask_b32_dpp v68, v84, v241, vcc row_ror:8 row_mask:0xf bank_mask:0xf\n"
      "v_mfma_f32_16x16x32_bf16 a[68:71], a[176:179], v[36:39], a[68:71]\n"
      "ds_read_b32 v233, v13 offset:320\n"
      "v_mov_b32_dpp v84, v241 row_ror:8 row_mask:0xf bank_mask:0x3\n"
      "v_perm_b32 v243, v165, v161, s43\n"
      "v_mfma_f32_16x16x32_bf16 a[72:75], a[176:179], v[40:43], a[72:75]\n"
      "buffer_load_dwordx4 v[156:159], v10, s[68:71], s40 offen offset:3072\n"
      "v_mfma_f32_16x16x32_bf16 a[76:79], a[176:179], v[44:47], a[76:79]\n"
      "v_perm_b32 v92, v167, v163, s43\n"
      "v_cndmask_b32_dpp v72, v88, v242, vcc row_ror:8 row_mask:0xf bank_mask:0xf\n"
      "s_waitcnt lgkmcnt(10)\n"
      "v_mfma_f32_16x16x32_bf16 a[80:83], a[180:183], v[32:35], a[80:83]\n"
      "ds_read_b128 a[172:175], v225\n"
      "v_mov_b32_dpp v88, v242 row_ror:8 row_mask:0xf bank_mask:0x3\n"
      "s_waitcnt vmcnt(22)\n"
      "v_perm_b32 v240, v172, v168, s42\n"
      "v_mfma_f32_16x16x32_bf16 a[84:87], a[180:183], v[36:39], a[84:87]\n"
      "global_load_dwordx2 a[248:249], v24, s[88:89] offset:0\n"
      "v_mfma_f32_16x16x32_bf16 a[88:91], a[180:183], v[40:43], a[88:91]\n"
      "ds_read_b32 v234, v14 offset:320\n"
      "v_perm_b32 v81, v174, v170, s42\n"
      "v_cndmask_b32_dpp v76, v92, v243, vcc row_ror:8 row_mask:0xf bank_mask:0xf\n"
      "v_mfma_f32_16x16x32_bf16 a[92:95], a[180:183], v[44:47], a[92:95]\n"
      "v_mov_b32_dpp v92, v243 row_ror:8 row_mask:0xf bank_mask:0x3\n"
      "v_perm_b32 v241, v172, v168, s43\n"
      "v_mfma_f32_16x16x32_bf16 a[96:99], a[128:131], v[32:35], a[96:99]\n"
      "ds_read_b128 a[176:179], v226\n"
      "v_perm_b32 v85, v174, v170, s43\n"
      "v_cndmask_b32_dpp v65, v81, v240, vcc row_ror:8 row_mask:0xf bank_mask:0xf\n"
      "v_mfma_f32_16x16x32_bf16 a[100:103], a[128:131], v[36:39], a[100:103]\n"
      "ds_read_b32 v235, v15 offset:320\n"
      "v_mov_b32_dpp v81, v240 row_ror:8 row_mask:0xf bank_mask:0x3\n"
      "v_perm_b32 v242, v173, v169, s42\n"
      "v_mfma_f32_16x16x32_bf16 a[104:107], a[128:131], v[40:43], a[104:107]\n"
      "global_load_dwordx2 a[250:251], v24, s[88:89] offset:16\n"
      "v_mfma_f32_16x16x32_bf16 a[108:111], a[128:131], v[44:47], a[108:111]\n"
      "v_perm_b32 v89, v175, v171, s42\n"
      "v_cndmask_b32_dpp v69, v85, v241, vcc row_ror:8 row_mask:0xf bank_mask:0xf\n"
      "v_mfma_f32_16x16x32_bf16 a[112:115], a[132:135], v[32:35], a[112:115]\n"
      "ds_read_b128 a[180:183], v227\n"
      "v_mov_b32_dpp v85, v241 row_ror:8 row_mask:0xf bank_mask:0x3\n"
      "v_perm_b32 v243, v173, v169, s43\n"
      "v_mfma_f32_16x16x32_bf16 a[116:119], a[132:135], v[36:39], a[116:119]\n"
      "ds_read_b32 v236, v12 offset:25920\n"
      "v_perm_b32 v93, v175, v171, s43\n"
      "v_cndmask_b32_dpp v73, v89, v242, vcc row_ror:8 row_mask:0xf bank_mask:0xf\n"
      "v_mfma_f32_16x16x32_bf16 a[120:123], a[132:135], v[40:43], a[120:123]\n"
      "ds_read_b32 v237, v13 offset:25920\n"
      "v_mov_b32_dpp v89, v242 row_ror:8 row_mask:0xf bank_mask:0x3\n"
      "s_waitcnt vmcnt(22)\n"
      "v_perm_b32 v240, v180, v176, s42\n"
      "v_mfma_f32_16x16x32_bf16 a[124:127], a[132:135], v[44:47], a[124:127]\n"
      "v_perm_b32 v82, v182, v178, s42\n"
      "v_cndmask_b32_dpp v77, v93, v243, vcc row_ror:8 row_mask:0xf bank_mask:0xf\n"
      "v_mfma_f32_16x16x32_bf16 a[0:3], a[136:139], v[48:51], a[0:3]\n"
      "ds_read_b128 a[128:131], v224 offset:16\n"
      "v_mov_b32_dpp v93, v243 row_ror:8 row_mask:0xf bank_mask:0x3\n"
      "v_perm_b32 v241, v180, v176, s43\n"
      "v_mfma_f32_16x16x32_bf16 a[4:7], a[136:139], v[52:55], a[4:7]\n"
      "global_load_dwordx2 a[252:253], v24, s[90:91] offset:0\n"
      "v_mfma_f32_16x16x32_bf16 a[8:11], a[136:139], v[56:59], a[8:11]\n"
      "ds_read_b32 v238, v14 offset:25920\n"
      "v_perm_b32 v86, v182, v178, s43\n"
      "v_cndmask_b32_dpp v66, v82, v240, vcc row_ror:8 row_mask:0xf bank_mask:0xf\n"
      "v_mfma_f32_16x16x32_bf16 a[12:15], a[136:139], v[60:63], a[12:15]\n"
      "v_mov_b32_dpp v82, v240 row_ror:8 row_mask:0xf bank_mask:0x3\n"
      "v_perm_b32 v242, v181, v177, s42\n"
      "s_waitcnt lgkmcnt(15)\n"
      "v_mfma_f32_16x16x32_bf16 a[16:19], a[140:143], v[48:51], a[16:19]\n"
      "ds_read_b128 a[132:135], v225 offset:16\n"
      "v_perm_b32 v90, v183, v179, s42\n"
      "v_cndmask_b32_dpp v70, v86, v241, vcc row_ror:8 row_mask:0xf bank_mask:0xf\n"
      "v_mfma_f32_16x16x32_bf16 a[20:23], a[140:143], v[52:55], a[20:23]\n"
      "ds_read_b32 v239, v15 offset:25920\n"
      "v_mov_b32_dpp v86, v241 row_ror:8 row_mask:0xf bank_mask:0x3\n"
      "v_perm_b32 v243, v181, v177, s43\n"
      "v_mfma_f32_16x16x32_bf16 a[24:27], a[140:143], v[56:59], a[24:27]\n"
      "global_load_dwordx2 a[254:255], v24, s[90:91] offset:16\n"
      "v_mfma_f32_16x16x32_bf16 a[28:31], a[140:143], v[60:63], a[28:31]\n"
      "v_perm_b32 v94, v183, v179, s43\n"
      "v_cndmask_b32_dpp v74, v90, v242, vcc row_ror:8 row_mask:0xf bank_mask:0xf\n"
      "v_mfma_f32_16x16x32_bf16 a[32:35], a[144:147], v[48:51], a[32:35]\n"
      "ds_read_b128 a[136:139], v226 offset:16\n"
      "v_mov_b32_dpp v90, v242 row_ror:8 row_mask:0xf bank_mask:0x3\n"
      "s_waitcnt vmcnt(22)\n"
      "v_perm_b32 v240, v188, v184, s42\n"
      "v_mfma_f32_16x16x32_bf16 a[36:39], a[144:147], v[52:55], a[36:39]\n"
      "v_perm_b32 v83, v190, v186, s42\n"
      "v_cndmask_b32_dpp v78, v94, v243, vcc row_ror:8 row_mask:0xf bank_mask:0xf\n"
      "v_mfma_f32_16x16x32_bf16 a[40:43], a[144:147], v[56:59], a[40:43]\n"
      "v_mov_b32_dpp v94, v243 row_ror:8 row_mask:0xf bank_mask:0x3\n"
      "v_perm_b32 v241, v188, v184, s43\n"
      "v_mfma_f32_16x16x32_bf16 a[44:47], a[144:147], v[60:63], a[44:47]\n"
      "v_perm_b32 v87, v190, v186, s43\n"
      "v_cndmask_b32_dpp v67, v83, v240, vcc row_ror:8 row_mask:0xf bank_mask:0xf\n"
      "v_mfma_f32_16x16x32_bf16 a[48:51], a[148:151], v[48:51], a[48:51]\n"
      "ds_read_b128 a[140:143], v227 offset:16\n"
      "v_mov_b32_dpp v83, v240 row_ror:8 row_mask:0xf bank_mask:0x3\n"
      "v_perm_b32 v242, v189, v185, s42\n"
      "v_mfma_f32_16x16x32_bf16 a[52:55], a[148:151], v[52:55], a[52:55]\n"
      "global_load_ushort v25, v25, s[80:81]\n"
      "v_mfma_f32_16x16x32_bf16 a[56:59], a[148:151], v[56:59], a[56:59]\n"
      "v_perm_b32 v91, v191, v187, s42\n"
      "v_cndmask_b32_dpp v71, v87, v241, vcc row_ror:8 row_mask:0xf bank_mask:0xf\n"
      "v_mfma_f32_16x16x32_bf16 a[60:63], a[148:151], v[60:63], a[60:63]\n"
      "v_mov_b32_dpp v87, v241 row_ror:8 row_mask:0xf bank_mask:0x3\n"
      "v_perm_b32 v243, v189, v185, s43\n"
      "v_mfma_f32_16x16x32_bf16 a[64:67], a[152:155], v[48:51], a[64:67]\n"
      "ds_read_b128 a[144:147], v224 offset:32\n"
      "v_perm_b32 v95, v191, v187, s43\n"
      "v_cndmask_b32_dpp v75, v91, v242, vcc row_ror:8 row_mask:0xf bank_mask:0xf\n"
      "v_mfma_f32_16x16x32_bf16 a[68:71], a[152:155], v[52:55], a[68:71]\n"
      "v_mov_b32_dpp v91, v242 row_ror:8 row_mask:0xf bank_mask:0x3\n"
      "v_cndmask_b32_dpp v79, v95, v243, vcc row_ror:8 row_mask:0xf bank_mask:0xf\n"
      "v_mfma_f32_16x16x32_bf16 a[72:75], a[152:155], v[56:59], a[72:75]\n"
      "global_load_ushort v26, v26, s[82:83]\n"
      "v_mfma_f32_16x16x32_bf16 a[76:79], a[152:155], v[60:63], a[76:79]\n"
      "v_mov_b32_dpp v95, v243 row_ror:8 row_mask:0xf bank_mask:0x3\n"
      "s_waitcnt vmcnt(21)\n"
      "v_perm_b32 v240, v196, v192, s42\n"
      "s_waitcnt lgkmcnt(15)\n"
      "v_mfma_f32_16x16x32_bf16 a[80:83], a[156:159], v[48:51], a[80:83]\n"
      "ds_read_b128 a[148:151], v225 offset:32\n"
      "v_perm_b32 v176, v198, v194, s42\n"
      "v_perm_b32 v241, v196, v192, s43\n"
      "v_mfma_f32_16x16x32_bf16 a[84:87], a[156:159], v[52:55], a[84:87]\n"
      "v_perm_b32 v180, v198, v194, s43\n"
      "v_cndmask_b32_dpp v160, v176, v240, vcc row_ror:8 row_mask:0xf bank_mask:0xf\n"
      "v_mfma_f32_16x16x32_bf16 a[88:91], a[156:159], v[56:59], a[88:91]\n"
      "v_mov_b32_dpp v176, v240 row_ror:8 row_mask:0xf bank_mask:0x3\n"
      "v_perm_b32 v242, v197, v193, s42\n"
      "v_mfma_f32_16x16x32_bf16 a[92:95], a[156:159], v[60:63], a[92:95]\n"
      "v_perm_b32 v184, v199, v195, s42\n"
      "v_cndmask_b32_dpp v164, v180, v241, vcc row_ror:8 row_mask:0xf bank_mask:0xf\n"
      "v_mfma_f32_16x16x32_bf16 a[96:99], a[160:163], v[48:51], a[96:99]\n"
      "ds_read_b128 a[152:155], v226 offset:32\n"
      "v_mov_b32_dpp v180, v241 row_ror:8 row_mask:0xf bank_mask:0x3\n"
      "v_perm_b32 v243, v197, v193, s43\n"
      "v_mfma_f32_16x16x32_bf16 a[100:103], a[160:163], v[52:55], a[100:103]\n"
      "global_load_ushort v27, v27, s[84:85]\n"
      "v_mfma_f32_16x16x32_bf16 a[104:107], a[160:163], v[56:59], a[104:107]\n"
      "s_waitcnt vmcnt(17)\n"
      "v_add_u32 v250, 0x9e3779b9, v250\n"
      "s_mov_b64 exec, 1\n"
      "buffer_store_dwordx2 v[250:251], v30, s[76:79], 0 offen sc1\n"
      "s_mov_b64 exec, -1\n"
      "v_mfma_f32_16x16x32_bf16 a[108:111], a[160:163], v[60:63], a[108:111]\n"
      "v_perm_b32 v188, v199, v195, s43\n"
      "v_cndmask_b32_dpp v168, v184, v242, vcc row_ror:8 row_mask:0xf bank_mask:0xf\n"
      "v_mfma_f32_16x16x32_bf16 a[112:115], a[164:167], v[48:51], a[112:115]\n"
      "ds_read_b128 a[156:159], v224 offset:48\n"
      "v_mov_b32_dpp v184, v242 row_ror:8 row_mask:0xf bank_mask:0x3\n"
      "v_perm_b32 v240, v204, v200, s42\n"
      "v_mfma_f32_16x16x32_bf16 a[116:119], a[164:167], v[52:55], a[116:119]\n"
      "v_mul_u32_u24 v232, s46, v232\n"
      "v_perm_b32 v177, v206, v202, s42\n"
      "v_mfma_f32_16x16x32_bf16 a[120:123], a[164:167], v[56:59], a[120:123]\n"
      "global_load_ushort v28, v28, s[92:93]\n"
      "v_mfma_f32_16x16x32_bf16 a[124:127], a[164:167], v[60:63], a[124:127]\n"
      "v_cndmask_b32_dpp v172, v188, v243, vcc row_ror:8 row_mask:0xf bank_mask:0xf\n"
      "v_mov_b32_dpp v188, v243 row_ror:8 row_mask:0xf bank_mask:0x3\n"
      "v_mfma_f32_16x16x32_bf16 a[0:3], a[168:171], v[96:99], a[0:3]\n"
      "ds_read_b128 a[160:163], v225 offset:48\n"
      "v_perm_b32 v241, v204, v200, s43\n"
      "v_perm_b32 v181, v206, v202, s43\n"
      "v_mfma_f32_16x16x32_bf16 a[4:7], a[168:171], v[100:103], a[4:7]\n"
      "s_waitcnt lgkmcnt(15)\n"
      "v_mul_u32_u24 v233, s46, v233\n"
      "v_cndmask_b32_dpp v161, v177, v240, vcc row_ror:8 row_mask:0xf bank_mask:0xf\n"
      "v_mfma_f32_16x16x32_bf16 a[8:11], a[168:171], v[104:107], a[8:11]\n"
      "v_mul_u32_u24 v234, s46, v234\n"
      "v_mov_b32_dpp v177, v240 row_ror:8 row_mask:0xf bank_mask:0x3\n"
      "v_mfma_f32_16x16x32_bf16 a[12:15], a[168:171], v[108:111], a[12:15]\n"
      "v_perm_b32 v242, v205, v201, s42\n"
      "v_perm_b32 v185, v207, v203, s42\n"
      "v_mfma_f32_16x16x32_bf16 a[16:19], a[172:175], v[96:99], a[16:19]\n"
      "ds_read_b128 a[164:167], v226 offset:48\n"
      "v_cndmask_b32_dpp v165, v181, v241, vcc row_ror:8 row_mask:0xf bank_mask:0xf\n"
      "v_mov_b32_dpp v181, v241 row_ror:8 row_mask:0xf bank_mask:0x3\n"
      "v_mfma_f32_16x16x32_bf16 a[20:23], a[172:175], v[100:103], a[20:23]\n"
      "global_load_dword a236, v8, s[94:95]\n"
      "v_mfma_f32_16x16x32_bf16 a[24:27], a[172:175], v[104:107], a[24:27]\n"
      "s_waitcnt lgkmcnt(15)\n"
      "v_mul_u32_u24 v235, s46, v235\n"
      "v_perm_b32 v243, v205, v201, s43\n"
      "v_mfma_f32_16x16x32_bf16 a[28:31], a[172:175], v[108:111], a[28:31]\n"
      "v_perm_b32 v189, v207, v203, s43\n"
      "v_cndmask_b32_dpp v169, v185, v242, vcc row_ror:8 row_mask:0xf bank_mask:0xf\n"
      "v_mfma_f32_16x16x32_bf16 a[32:35], a[176:179], v[96:99], a[32:35]\n"
      "v_mov_b32_dpp v185, v242 row_ror:8 row_mask:0xf bank_mask:0x3\n"
      "v_perm_b32 v240, v212, v208, s42\n"
      "v_mfma_f32_16x16x32_bf16 a[36:39], a[176:179], v[100:103], a[36:39]\n"
      "s_waitcnt lgkmcnt(13)\n"
      "v_mul_u32_u24 v236, s46, v236\n"
      "v_perm_b32 v178, v214, v210, s42\n"
      "v_mfma_f32_16x16x32_bf16 a[40:43], a[176:179], v[104:107], a[40:43]\n"
      "s_waitcnt lgkmcnt(12)\n"
      "v_mul_u32_u24 v237, s46, v237\n"
      "v_cndmask_b32_dpp v173, v189, v243, vcc row_ror:8 row_mask:0xf bank_mask:0xf\n"
      "v_mfma_f32_16x16x32_bf16 a[44:47], a[176:179], v[108:111], a[44:47]\n"
      "v_mov_b32_dpp v189, v243 row_ror:8 row_mask:0xf bank_mask:0x3\n"
      "v_perm_b32 v241, v212, v208, s43\n"
      "v_mfma_f32_16x16x32_bf16 a[48:51], a[180:183], v[96:99], a[48:51]\n"
      "v_perm_b32 v182, v214, v210, s43\n"
      "v_cndmask_b32_dpp v162, v178, v240, vcc row_ror:8 row_mask:0xf bank_mask:0xf\n"
      "v_mfma_f32_16x16x32_bf16 a[52:55], a[180:183], v[100:103], a[52:55]\n"
      "s_waitcnt lgkmcnt(10)\n"
      "v_mul_u32_u24 v238, s46, v238\n"
      "v_mov_b32_dpp v178, v240 row_ror:8 row_mask:0xf bank_mask:0x3\n"
      "v_mfma_f32_16x16x32_bf16 a[56:59], a[180:183], v[104:107], a[56:59]\n"
      "s_waitcnt lgkmcnt(8)\n"
      "v_mul_u32_u24 v239, s46, v239\n"
      "v_perm_b32 v242, v213, v209, s42\n"
      "v_mfma_f32_16x16x32_bf16 a[60:63], a[180:183], v[108:111], a[60:63]\n"
      "v_perm_b32 v186, v215, v211, s42\n"
      "v_cndmask_b32_dpp v166, v182, v241, vcc row_ror:8 row_mask:0xf bank_mask:0xf\n"
      "v_mfma_f32_16x16x32_bf16 a[0:3], a[128:131], v[112:115], a[0:3]\n"
      "v_mov_b32_dpp v182, v241 row_ror:8 row_mask:0xf bank_mask:0x3\n"
      "v_perm_b32 v243, v213, v209, s43\n"
      "v_mfma_f32_16x16x32_bf16 a[4:7], a[128:131], v[116:119], a[4:7]\n"
      "v_and_b32 v232, -16, v232\n"
      "v_perm_b32 v190, v215, v211, s43\n"
      "v_mfma_f32_16x16x32_bf16 a[8:11], a[128:131], v[120:123], a[8:11]\n"
      "v_and_b32 v233, -16, v233\n"
      "v_cndmask_b32_dpp v170, v186, v242, vcc row_ror:8 row_mask:0xf bank_mask:0xf\n"
      "v_mfma_f32_16x16x32_bf16 a[12:15], a[128:131], v[124:127], a[12:15]\n"
      "v_mov_b32_dpp v186, v242 row_ror:8 row_mask:0xf bank_mask:0x3\n"
      "s_waitcnt vmcnt(18)\n"
      "v_perm_b32 v240, v220, v216, s42\n"
      "v_mfma_f32_16x16x32_bf16 a[16:19], a[132:135], v[112:115], a[16:19]\n"
      "v_perm_b32 v179, v222, v218, s42\n"
      "v_cndmask_b32_dpp v174, v190, v243, vcc row_ror:8 row_mask:0xf bank_mask:0xf\n"
      "v_mfma_f32_16x16x32_bf16 a[20:23], a[132:135], v[116:119], a[20:23]\n"
      "v_and_b32 v234, -16, v234\n"
      "v_mov_b32_dpp v190, v243 row_ror:8 row_mask:0xf bank_mask:0x3\n"
      "v_mfma_f32_16x16x32_bf16 a[24:27], a[132:135], v[120:123], a[24:27]\n"
      "v_and_b32 v235, -16, v235\n"
      "v_perm_b32 v241, v220, v216, s43\n"
      "v_mfma_f32_16x16x32_bf16 a[28:31], a[132:135], v[124:127], a[28:31]\n"
      "v_perm_b32 v183, v222, v218, s43\n"
      "v_cndmask_b32_dpp v163, v179, v240, vcc row_ror:8 row_mask:0xf bank_mask:0xf\n"
      "s_waitcnt lgkmcnt(4)\n"
      "v_mfma_f32_16x16x32_bf16 a[32:35], a[136:139], v[112:115], a[32:35]\n"
      "v_mov_b32_dpp v179, v240 row_ror:8 row_mask:0xf bank_mask:0x3\n"
      "v_perm_b32 v242, v221, v217, s42\n"
      "v_mfma_f32_16x16x32_bf16 a[36:39], a[136:139], v[116:119], a[36:39]\n"
      "v_and_b32 v236, -16, v236\n"
      "v_perm_b32 v187, v223, v219, s42\n"
      "v_mfma_f32_16x16x32_bf16 a[40:43], a[136:139], v[120:123], a[40:43]\n"
      "v_and_b32 v237, -16, v237\n"
      "v_cndmask_b32_dpp v167, v183, v241, vcc row_ror:8 row_mask:0xf bank_mask:0xf\n"
      "v_mfma_f32_16x16x32_bf16 a[44:47], a[136:139], v[124:127], a[44:47]\n"
      "v_mov_b32_dpp v183, v241 row_ror:8 row_mask:0xf bank_mask:0x3\n"
      "v_perm_b32 v243, v221, v217, s43\n"
      "v_mfma_f32_16x16x32_bf16 a[48:51], a[140:143], v[112:115], a[48:51]\n"
      "v_and_b32 v238, -16, v238\n"
      "v_and_b32 v239, -16, v239\n"
      "ds_read_b128 a[168:171], v232\n"
      "s_barrier\n"
      "v_mfma_f32_16x16x32_bf16 a[52:55], a[140:143], v[116:119], a[52:55]\n"
      "v_perm_b32 v191, v223, v219, s43\n"
      "v_cndmask_b32_dpp v171, v187, v242, vcc row_ror:8 row_mask:0xf bank_mask:0xf\n"
      "v_mfma_f32_16x16x32_bf16 a[56:59], a[140:143], v[120:123], a[56:59]\n"
      "ds_read_b128 a[172:175], v232 offset:16\n"
      "v_mov_b32_dpp v187, v242 row_ror:8 row_mask:0xf bank_mask:0x3\n"
      "v_cndmask_b32_dpp v175, v191, v243, vcc row_ror:8 row_mask:0xf bank_mask:0xf\n"
      "v_mfma_f32_16x16x32_bf16 a[60:63], a[140:143], v[124:127], a[60:63]\n"
      "v_mov_b32_dpp v191, v243 row_ror:8 row_mask:0xf bank_mask:0x3\n"
      "s_waitcnt vmcnt(16)\n"
      "v_perm_b32 v240, v132, v128, s42\n"
      "v_mfma_f32_16x16x32_bf16 a[0:3], a[144:147], v[64:67], a[0:3]\n"
      "ds_read_b128 a[176:179], v232 offset:32\n"
      "v_perm_b32 v48, v134, v130, s42\n"
      "v_perm_b32 v241, v132, v128, s43\n"
      "v_mfma_f32_16x16x32_bf16 a[4:7], a[144:147], v[68:71], a[4:7]\n"
      "v_perm_b32 v52, v134, v130, s43\n"
      "v_cndmask_b32_dpp v32, v48, v240, vcc row_ror:8 row_mask:0xf bank_mask:0xf\n"
      "v_mfma_f32_16x16x32_bf16 a[8:11], a[144:147], v[72:75], a[8:11]\n"
      "ds_read_b128 a[180:183], v232 offset:48\n"
      "v_mov_b32_dpp v48, v240 row_ror:8 row_mask:0xf bank_mask:0x3\n"
      "v_perm_b32 v242, v133, v129, s42\n"
      "v_mfma_f32_16x16x32_bf16 a[12:15], a[144:147], v[76:79], a[12:15]\n"
      "v_perm_b32 v56, v135, v131, s42\n"
      "v_cndmask_b32_dpp v36, v52, v241, vcc row_ror:8 row_mask:0xf bank_mask:0xf\n"
      "v_mfma_f32_16x16x32_bf16 a[16:19], a[148:151], v[64:67], a[16:19]\n"
      "ds_read_b128 a[128:131], v233\n"
      "v_mov_b32_dpp v52, v241 row_ror:8 row_mask:0xf bank_mask:0x3\n"
      "v_perm_b32 v243, v133, v129, s43\n"
      "v_mfma_f32_16x16x32_bf16 a[20:23], a[148:151], v[68:71], a[20:23]\n"
      "v_perm_b32 v60, v135, v131, s43\n"
      "v_cndmask_b32_dpp v40, v56, v242, vcc row_ror:8 row_mask:0xf bank_mask:0xf\n"
      "v_mfma_f32_16x16x32_bf16 a[24:27], a[148:151], v[72:75], a[24:27]\n"
      "ds_read_b128 a[132:135], v233 offset:16\n"
      "v_mov_b32_dpp v56, v242 row_ror:8 row_mask:0xf bank_mask:0x3\n"
      "s_waitcnt vmcnt(14)\n"
      "v_perm_b32 v240, v140, v136, s42\n"
      "v_mfma_f32_16x16x32_bf16 a[28:31], a[148:151], v[76:79], a[28:31]\n"
      "v_perm_b32 v49, v142, v138, s42\n"
      "v_cndmask_b32_dpp v44, v60, v243, vcc row_ror:8 row_mask:0xf bank_mask:0xf\n"
      "s_waitcnt lgkmcnt(6)\n"
      "v_mfma_f32_16x16x32_bf16 a[32:35], a[152:155], v[64:67], a[32:35]\n"
      "ds_read_b128 a[136:139], v233 offset:32\n"
      "v_mov_b32_dpp v60, v243 row_ror:8 row_mask:0xf bank_mask:0x3\n"
      "v_perm_b32 v241, v140, v136, s43\n"
      "v_mfma_f32_16x16x32_bf16 a[36:39], a[152:155], v[68:71], a[36:39]\n"
      "v_perm_b32 v53, v142, v138, s43\n"
      "v_cndmask_b32_dpp v33, v49, v240, vcc row_ror:8 row_mask:0xf bank_mask:0xf\n"
      "v_mfma_f32_16x16x32_bf16 a[40:43], a[152:155], v[72:75], a[40:43]\n"
      "ds_read_b128 a[140:143], v233 offset:48\n"
      "v_mov_b32_dpp v49, v240 row_ror:8 row_mask:0xf bank_mask:0x3\n"
      "v_perm_b32 v242, v141, v137, s42\n"
      "v_mfma_f32_16x16x32_bf16 a[44:47], a[152:155], v[76:79], a[44:47]\n"
      "v_perm_b32 v57, v143, v139, s42\n"
      "v_cndmask_b32_dpp v37, v53, v241, vcc row_ror:8 row_mask:0xf bank_mask:0xf\n"
      "v_mfma_f32_16x16x32_bf16 a[0:3], a[156:159], v[80:83], a[0:3]\n"
      "ds_read_b128 a[144:147], v234\n"
      "v_mov_b32_dpp v53, v241 row_ror:8 row_mask:0xf bank_mask:0x3\n"
      "v_perm_b32 v243, v141, v137, s43\n"
      "v_mfma_f32_16x16x32_bf16 a[4:7], a[156:159], v[84:87], a[4:7]\n"
      "v_perm_b32 v61, v143, v139, s43\n"
      "v_cndmask_b32_dpp v41, v57, v242, vcc row_ror:8 row_mask:0xf bank_mask:0xf\n"
      "v_mfma_f32_16x16x32_bf16 a[8:11], a[156:159], v[88:91], a[8:11]\n"
      "ds_read_b128 a[148:151], v234 offset:16\n"
      "v_mov_b32_dpp v57, v242 row_ror:8 row_mask:0xf bank_mask:0x3\n"
      "s_waitcnt vmcnt(12)\n"
      "v_perm_b32 v240, v148, v144, s42\n"
      "v_mfma_f32_16x16x32_bf16 a[12:15], a[156:159], v[92:95], a[12:15]\n"
      "v_perm_b32 v50, v150, v146, s42\n"
      "v_cndmask_b32_dpp v45, v61, v243, vcc row_ror:8 row_mask:0xf bank_mask:0xf\n"
      "v_mfma_f32_16x16x32_bf16 a[16:19], a[160:163], v[80:83], a[16:19]\n"
      "ds_read_b128 a[152:155], v234 offset:32\n"
      "v_mov_b32_dpp v61, v243 row_ror:8 row_mask:0xf bank_mask:0x3\n"
      "v_perm_b32 v241, v148, v144, s43\n"
      "v_mfma_f32_16x16x32_bf16 a[20:23], a[160:163], v[84:87], a[20:23]\n"
      "v_perm_b32 v54, v150, v146, s43\n"
      "v_cndmask_b32_dpp v34, v50, v240, vcc row_ror:8 row_mask:0xf bank_mask:0xf\n"
      "v_mfma_f32_16x16x32_bf16 a[24:27], a[160:163], v[88:91], a[24:27]\n"
      "v_mov_b32_dpp v50, v240 row_ror:8 row_mask:0xf bank_mask:0x3\n"
      "v_perm_b32 v242, v149, v145, s42\n"
      "v_mfma_f32_16x16x32_bf16 a[28:31], a[160:163], v[92:95], a[28:31]\n"
      "v_perm_b32 v58, v151, v147, s42\n"
      "v_cndmask_b32_dpp v38, v54, v241, vcc row_ror:8 row_mask:0xf bank_mask:0xf\n"
      "v_mfma_f32_16x16x32_bf16 a[32:35], a[164:167], v[80:83], a[32:35]\n"
      "ds_read_b128 a[156:159], v234 offset:48\n"
      "v_mov_b32_dpp v54, v241 row_ror:8 row_mask:0xf bank_mask:0x3\n"
      "v_perm_b32 v243, v149, v145, s43\n"
      "v_mfma_f32_16x16x32_bf16 a[36:39], a[164:167], v[84:87], a[36:39]\n"
      "v_perm_b32 v62, v151, v147, s43\n"
      "v_cndmask_b32_dpp v42, v58, v242, vcc row_ror:8 row_mask:0xf bank_mask:0xf\n"
      "v_mfma_f32_16x16x32_bf16 a[40:43], a[164:167], v[88:91], a[40:43]\n"
      "v_mov_b32_dpp v58, v242 row_ror:8 row_mask:0xf bank_mask:0x3\n"
      "s_waitcnt vmcnt(10)\n"
      "v_perm_b32 v240, v156, v152, s42\n"
      "v_mfma_f32_16x16x32_bf16 a[44:47], a[164:167], v[92:95], a[44:47]\n"
      "v_perm_b32 v51, v158, v154, s42\n"
      "v_cndmask_b32_dpp v46, v62, v243, vcc row_ror:8 row_mask:0xf bank_mask:0xf\n"
      "s_waitcnt lgkmcnt(8)\n"
      "v_mfma_f32_16x16x32_bf16 a[0:3], a[168:171], v[160:163], a[0:3]\n"
      "ds_read_b128 a[160:163], v227 offset:32\n"
      "v_mov_b32_dpp v62, v243 row_ror:8 row_mask:0xf bank_mask:0x3\n"
      "v_perm_b32 v241, v156, v152, s43\n"
      "v_mfma_f32_16x16x32_bf16 a[4:7], a[168:171], v[164:167], a[4:7]\n"
      "v_perm_b32 v55, v158, v154, s43\n"
      "v_cndmask_b32_dpp v35, v51, v240, vcc row_ror:8 row_mask:0xf bank_mask:0xf\n"
      "v_mfma_f32_16x16x32_bf16 a[8:11], a[168:171], v[168:171], a[8:11]\n"
      "v_mov_b32_dpp v51, v240 row_ror:8 row_mask:0xf bank_mask:0x3\n"
      "v_perm_b32 v242, v157, v153, s42\n"
      "v_mfma_f32_16x16x32_bf16 a[12:15], a[168:171], v[172:175], a[12:15]\n"
      "v_perm_b32 v59, v159, v155, s42\n"
      "v_cndmask_b32_dpp v39, v55, v241, vcc row_ror:8 row_mask:0xf bank_mask:0xf\n"
      "v_mfma_f32_16x16x32_bf16 a[0:3], a[172:175], v[176:179], a[0:3]\n"
      "ds_read_b128 a[164:167], v227 offset:48\n"
      "v_mov_b32_dpp v55, v241 row_ror:8 row_mask:0xf bank_mask:0x3\n"
      "v_perm_b32 v243, v157, v153, s43\n"
      "v_mfma_f32_16x16x32_bf16 a[4:7], a[172:175], v[180:183], a[4:7]\n"
      "v_perm_b32 v63, v159, v155, s43\n"
      "v_cndmask_b32_dpp v43, v59, v242, vcc row_ror:8 row_mask:0xf bank_mask:0xf\n"
      "v_mfma_f32_16x16x32_bf16 a[8:11], a[172:175], v[184:187], a[8:11]\n"
      "v_mov_b32_dpp v59, v242 row_ror:8 row_mask:0xf bank_mask:0x3\n"
      "v_cndmask_b32_dpp v47, v63, v243, vcc row_ror:8 row_mask:0xf bank_mask:0xf\n"
      "v_mfma_f32_16x16x32_bf16 a[12:15], a[172:175], v[188:191], a[12:15]\n"
      "v_mov_b32_dpp v63, v243 row_ror:8 row_mask:0xf bank_mask:0x3\n"
      "v_mfma_f32_16x16x32_bf16 a[0:3], a[176:179], v[32:35], a[0:3]\n"
      "ds_read_b128 a[168:171], v235\n"
      "v_mfma_f32_16x16x32_bf16 a[4:7], a[176:179], v[36:39], a[4:7]\n"
      "v_accvgpr_read_b32 v192, a48\n"
      "v_accvgpr_read_b32 v193, a49\n"
      "v_mfma_f32_16x16x32_bf16 a[8:11], a[176:179], v[40:43], a[8:11]\n"
      "v_accvgpr_read_b32 v194, a50\n"
      "v_accvgpr_read_b32 v195, a51\n"
      "v_mfma_f32_16x16x32_bf16 a[12:15], a[176:179], v[44:47], a[12:15]\n"
      "v_accvgpr_read_b32 v196, a52\n"
      "v_accvgpr_read_b32 v197, a53\n"
      "v_mfma_f32_16x16x32_bf16 a[0:3], a[180:183], v[48:51], a[0:3]\n"
      "ds_read_b128 a[172:175], v235 offset:16\n"
      "v_accvgpr_read_b32 v198, a54\n"
      "v_accvgpr_read_b32 v199, a55\n"
      "v_mfma_f32_16x16x32_bf16 a[4:7], a[180:183], v[52:55], a[4:7]\n"
      "v_accvgpr_read_b32 v200, a56\n"
      "v_accvgpr_read_b32 v201, a57\n"
      "v_mfma_f32_16x16x32_bf16 a[8:11], a[180:183], v[56:59], a[8:11]\n"
      "v_accvgpr_read_b32 v202, a58\n"
      "v_accvgpr_read_b32 v203, a59\n"
      "v_mfma_f32_16x16x32_bf16 a[12:15], a[180:183], v[60:63], a[12:15]\n"
      "v_accvgpr_read_b32 v204, a60\n"
      "v_accvgpr_read_b32 v205, a61\n"
      "s_waitcnt lgkmcnt(8)\n"
      "v_mfma_f32_16x16x32_bf16 a[16:19], a[128:131], v[160:163], a[16:19]\n"
      "ds_read_b128 a[176:179], v235 offset:32\n"
      "v_accvgpr_read_b32 v206, a62\n"
      "v_accvgpr_read_b32 v207, a63\n"
      "v_mfma_f32_16x16x32_bf16 a[20:23], a[128:131], v[164:167], a[20:23]\n"
      "ds_write_b128 v18, a[0:3] offset:49152\n"
      "v_mfma_f32_16x16x32_bf16 a[24:27], a[128:131], v[168:171], a[24:27]\n"
      "v_mfma_f32_16x16x32_bf16 a[28:31], a[128:131], v[172:175], a[28:31]\n"
      "v_mfma_f32_16x16x32_bf16 a[16:19], a[132:135], v[176:179], a[16:19]\n"
      "ds_read_b128 a[180:183], v235 offset:48\n"
      "v_mfma_f32_16x16x32_bf16 a[20:23], a[132:135], v[180:183], a[20:23]\n"
      "ds_write_b128 v18, a[4:7] offset:50176\n"
      "v_mfma_f32_16x16x32_bf16 a[24:27], a[132:135], v[184:187], a[24:27]\n"
      "v_mfma_f32_16x16x32_bf16 a[28:31], a[132:135], v[188:191], a[28:31]\n"
      "ds_write_b128 v18, a[8:11] offset:51200\n"
      "v_mfma_f32_16x16x32_bf16 a[16:19], a[136:139], v[32:35], a[16:19]\n"
      "ds_read_b128 a[128:131], v228\n"
      "v_mfma_f32_16x16x32_bf16 a[20:23], a[136:139], v[36:39], a[20:23]\n"
      "v_mfma_f32_16x16x32_bf16 a[24:27], a[136:139], v[40:43], a[24:27]\n"
      "ds_write_b128 v18, a[12:15] offset:52224\n"
      "v_mfma_f32_16x16x32_bf16 a[28:31], a[136:139], v[44:47], a[28:31]\n"
      "v_mfma_f32_16x16x32_bf16 a[16:19], a[140:143], v[48:51], a[16:19]\n"
      "ds_read_b128 a[132:135], v228 offset:16\n"
      "v_mfma_f32_16x16x32_bf16 a[20:23], a[140:143], v[52:55], a[20:23]\n"
      "v_mfma_f32_16x16x32_bf16 a[24:27], a[140:143], v[56:59], a[24:27]\n"
      "v_mfma_f32_16x16x32_bf16 a[28:31], a[140:143], v[60:63], a[28:31]\n"
      "s_waitcnt lgkmcnt(12)\n"
      "v_mfma_f32_16x16x32_bf16 a[32:35], a[144:147], v[160:163], a[32:35]\n"
      "ds_read_b128 a[136:139], v228 offset:32\n"
      "v_mfma_f32_16x16x32_bf16 a[36:39], a[144:147], v[164:167], a[36:39]\n"
      "ds_write_b128 v19, a[16:19] offset:53248\n"
      "v_mfma_f32_16x16x32_bf16 a[40:43], a[144:147], v[168:171], a[40:43]\n"
      "v_mfma_f32_16x16x32_bf16 a[44:47], a[144:147], v[172:175], a[44:47]\n"
      "v_mfma_f32_16x16x32_bf16 a[32:35], a[148:151], v[176:179], a[32:35]\n"
      "ds_read_b128 a[140:143], v228 offset:48\n"
      "v_mfma_f32_16x16x32_bf16 a[36:39], a[148:151], v[180:183], a[36:39]\n"
      "ds_write_b128 v19, a[20:23] offset:54272\n"
      "v_mfma_f32_16x16x32_bf16 a[40:43], a[148:151], v[184:187], a[40:43]\n"
      "v_mfma_f32_16x16x32_bf16 a[44:47], a[148:151], v[188:191], a[44:47]\n"
      "ds_write_b128 v19, a[24:27] offset:55296\n"
      "v_mfma_f32_16x16x32_bf16 a[32:35], a[152:155], v[32:35], a[32:35]\n"
      "ds_read_b128 a[144:147], v236\n"
      "v_mfma_f32_16x16x32_bf16 a[36:39], a[152:155], v[36:39], a[36:39]\n"
      "v_mfma_f32_16x16x32_bf16 a[40:43], a[152:155], v[40:43], a[40:43]\n"
      "ds_write_b128 v19, a[28:31] offset:56320\n"
      "v_mfma_f32_16x16x32_bf16 a[44:47], a[152:155], v[44:47], a[44:47]\n"
      "v_mfma_f32_16x16x32_bf16 a[32:35], a[156:159], v[48:51], a[32:35]\n"
      "ds_read_b128 a[148:151], v236 offset:16\n"
      "v_mfma_f32_16x16x32_bf16 a[36:39], a[156:159], v[52:55], a[36:39]\n"
      "v_mfma_f32_16x16x32_bf16 a[40:43], a[156:159], v[56:59], a[40:43]\n"
      "v_mfma_f32_16x16x32_bf16 a[44:47], a[156:159], v[60:63], a[44:47]\n"
      "s_waitcnt lgkmcnt(15)\n"
      "v_mfma_f32_16x16x32_bf16 v[192:195], a[160:163], v[64:67], v[192:195]\n"
      "ds_read_b128 a[152:155], v236 offset:32\n"
      "v_mfma_f32_16x16x32_bf16 v[196:199], a[160:163], v[68:71], v[196:199]\n"
      "ds_write_b128 v20, a[32:35] offset:57344\n"
      "v_mfma_f32_16x16x32_bf16 v[200:203], a[160:163], v[72:75], v[200:203]\n"
      "v_mfma_f32_16x16x32_bf16 v[204:207], a[160:163], v[76:79], v[204:207]\n"
      "v_mfma_f32_16x16x32_bf16 v[192:195], a[164:167], v[80:83], v[192:195]\n"
      "ds_read_b128 a[156:159], v236 offset:48\n"
      "v_mfma_f32_16x16x32_bf16 v[196:199], a[164:167], v[84:87], v[196:199]\n"
      "ds_write_b128 v20, a[36:39] offset:58368\n"
      "v_mfma_f32_16x16x32_bf16 v[200:203], a[164:167], v[88:91], v[200:203]\n"
      "v_mfma_f32_16x16x32_bf16 v[204:207], a[164:167], v[92:95], v[204:207]\n"
      "ds_write_b128 v20, a[40:43] offset:59392\n"
      "v_mfma_f32_16x16x32_bf16 v[192:195], a[168:171], v[160:163], v[192:195]\n"
      "ds_read_b128 a[160:163], v229\n"
      "v_mfma_f32_16x16x32_bf16 v[196:199], a[168:171], v[164:167], v[196:199]\n"
      "v_mfma_f32_16x16x32_bf16 v[200:203], a[168:171], v[168:171], v[200:203]\n"
      "ds_write_b128 v20, a[44:47] offset:60416\n"
      "v_mfma_f32_16x16x32_bf16 v[204:207], a[168:171], v[172:175], v[204:207]\n"
      "v_mfma_f32_16x16x32_bf16 v[192:195], a[172:175], v[176:179], v[192:195]\n"
      "ds_read_b128 a[164:167], v229 offset:16\n"
      "v_mfma_f32_16x16x32_bf16 v[196:199], a[172:175], v[180:183], v[196:199]\n"
      "s_waitcnt lgkmcnt(1)\n"
      "s_barrier\n"
      "ds_read_b128 v[208:211], v21 offset:49152\n"
      "ds_read_b128 v[212:215], v21 offset:50176\n"
      "v_mfma_f32_16x16x32_bf16 v[200:203], a[172:175], v[184:187], v[200:203]\n"
      "ds_read_b128 v[216:219], v21 offset:51200\n"
      "ds_read_b128 v[220:223], v21 offset:52224\n"
      "v_mfma_f32_16x16x32_bf16 v[204:207], a[172:175], v[188:191], v[204:207]\n"
      "ds_read_b128 v[128:131], v21 offset:53248\n"
      "ds_read_b128 v[132:135], v21 offset:54272\n"
      "v_mfma_f32_16x16x32_bf16 v[192:195], a[176:179], v[32:35], v[192:195]\n"
      "ds_read_b128 a[168:171], v229 offset:32\n"
      "ds_read_b128 v[136:139], v21 offset:55296\n"
      "ds_read_b128 v[140:143], v21 offset:56320\n"
      "v_mfma_f32_16x16x32_bf16 v[196:199], a[176:179], v[36:39], v[196:199]\n"
      "ds_read_b128 v[144:147], v21 offset:57344\n"
      "ds_read_b128 v[148:151], v21 offset:58368\n"
      "v_mfma_f32_16x16x32_bf16 v[200:203], a[176:179], v[40:43], v[200:203]\n"
      "ds_read_b128 v[152:155], v21 offset:59392\n"
      "ds_read_b128 v[156:159], v21 offset:60416\n"
      "v_mfma_f32_16x16x32_bf16 v[204:207], a[176:179], v[44:47], v[204:207]\n"
      "v_mfma_f32_16x16x32_bf16 v[192:195], a[180:183], v[48:51], v[192:195]\n"
      "ds_read_b128 a[172:175], v229 offset:48\n"
      "v_mfma_f32_16x16x32_bf16 v[196:199], a[180:183], v[52:55], v[196:199]\n"
      "s_waitcnt lgkmcnt(9)\n"
      "v_add_f32 v208, v208, v128\n"
      "v_add_f32 v209, v209, v129\n"
      "v_mfma_f32_16x16x32_bf16 v[200:203], a[180:183], v[56:59], v[200:203]\n"
      "v_add_f32 v210, v210, v130\n"
      "v_add_f32 v211, v211, v131\n"
      "v_mfma_f32_16x16x32_bf16 v[204:207], a[180:183], v[60:63], v[204:207]\n"
      "s_waitcnt lgkmcnt(8)\n"
      "v_add_f32 v212, v212, v132\n"
      "v_add_f32 v213, v213, v133\n"
      "v_mfma_f32_16x16x32_bf16 a[64:67], a[128:131], v[96:99], a[64:67]\n"
      "ds_read_b128 a[176:179], v237\n"
      "v_add_f32 v214, v214, v134\n"
      "v_add_f32 v215, v215, v135\n"
      "v_mfma_f32_16x16x32_bf16 a[68:71], a[128:131], v[100:103], a[68:71]\n"
      "s_waitcnt lgkmcnt(7)\n"
      "v_add_f32 v216, v216, v136\n"
      "v_add_f32 v217, v217, v137\n"
      "v_mfma_f32_16x16x32_bf16 a[72:75], a[128:131], v[104:107], a[72:75]\n"
      "v_add_f32 v218, v218, v138\n"
      "v_add_f32 v219, v219, v139\n"
      "v_mfma_f32_16x16x32_bf16 a[76:79], a[128:131], v[108:111], a[76:79]\n"
      "s_waitcnt lgkmcnt(6)\n"
      "v_add_f32 v220, v220, v140\n"
      "v_add_f32 v221, v221, v141\n"
      "v_mfma_f32_16x16x32_bf16 a[64:67], a[132:135], v[112:115], a[64:67]\n"
      "ds_read_b128 a[180:183], v237 offset:16\n"
      "v_add_f32 v222, v222, v142\n"
      "v_add_f32 v223, v223, v143\n"
      "v_mfma_f32_16x16x32_bf16 a[68:71], a[132:135], v[116:119], a[68:71]\n"
      "s_waitcnt lgkmcnt(6)\n"
      "v_add_f32 v208, v208, v144\n"
      "v_add_f32 v209, v209, v145\n"
      "v_mfma_f32_16x16x32_bf16 a[72:75], a[132:135], v[120:123], a[72:75]\n"
      "v_add_f32 v210, v210, v146\n"
      "v_add_f32 v211, v211, v147\n"
      "v_mfma_f32_16x16x32_bf16 a[76:79], a[132:135], v[124:127], a[76:79]\n"
      "s_waitcnt lgkmcnt(5)\n"
      "v_add_f32 v212, v212, v148\n"
      "v_add_f32 v213, v213, v149\n"
      "v_mfma_f32_16x16x32_bf16 a[64:67], a[136:139], v[64:67], a[64:67]\n"
      "ds_read_b128 a[128:131], v237 offset:32\n"
      "v_add_f32 v214, v214, v150\n"
      "v_add_f32 v215, v215, v151\n"
      "v_mfma_f32_16x16x32_bf16 a[68:71], a[136:139], v[68:71], a[68:71]\n"
      "s_waitcnt lgkmcnt(5)\n"
      "v_add_f32 v216, v216, v152\n"
      "v_add_f32 v217, v217, v153\n"
      "v_mfma_f32_16x16x32_bf16 a[72:75], a[136:139], v[72:75], a[72:75]\n"
      "v_add_f32 v218, v218, v154\n"
      "v_add_f32 v219, v219, v155\n"
      "v_mfma_f32_16x16x32_bf16 a[76:79], a[136:139], v[76:79], a[76:79]\n"
      "s_waitcnt lgkmcnt(4)\n"
      "v_add_f32 v220, v220, v156\n"
      "v_add_f32 v221, v221, v157\n"
      "v_mfma_f32_16x16x32_bf16 a[64:67], a[140:143], v[80:83], a[64:67]\n"
      "ds_read_b128 a[132:135], v237 offset:48\n"
      "v_add_f32 v222, v222, v158\n"
      "v_add_f32 v223, v223, v159\n"
      "v_mfma_f32_16x16x32_bf16 a[68:71], a[140:143], v[84:87], a[68:71]\n"
      "v_add_f32 v192, v192, v208\n"
      "v_add_f32 v193, v193, v209\n"
      "v_mfma_f32_16x16x32_bf16 a[72:75], a[140:143], v[88:91], a[72:75]\n"
      "v_add_f32 v196, v196, v212\n"
      "v_add_f32 v197, v197, v213\n"
      "v_mfma_f32_16x16x32_bf16 a[76:79], a[140:143], v[92:95], a[76:79]\n"
      "v_add_f32 v200, v200, v216\n"
      "v_add_f32 v201, v201, v217\n"
      "v_mfma_f32_16x16x32_bf16 a[64:67], a[144:147], v[160:163], a[64:67]\n"
      "ds_read_b128 a[136:139], v230\n"
      "v_add_f32 v204, v204, v220\n"
      "v_add_f32 v205, v205, v221\n"
      "v_mfma_f32_16x16x32_bf16 a[68:71], a[144:147], v[164:167], a[68:71]\n"
      "v_add_f32 v194, v194, v210\n"
      "v_add_f32 v195, v195, v211\n"
      "v_mfma_f32_16x16x32_bf16 a[72:75], a[144:147], v[168:171], a[72:75]\n"
      "v_add_f32 v198, v198, v214\n"
      "v_add_f32 v199, v199, v215\n"
      "v_mfma_f32_16x16x32_bf16 a[76:79], a[144:147], v[172:175], a[76:79]\n"
      "v_add_f32 v202, v202, v218\n"
      "v_add_f32 v203, v203, v219\n"
      "v_mfma_f32_16x16x32_bf16 a[64:67], a[148:151], v[176:179], a[64:67]\n"
      "ds_read_b128 a[140:143], v230 offset:16\n"
      "v_add_f32 v206, v206, v222\n"
      "v_add_f32 v207, v207, v223\n"
      "v_mfma_f32_16x16x32_bf16 a[68:71], a[148:151], v[180:183], a[68:71]\n"
      "v_cvt_pk_bf16_f32 v128, v192, v196\n"
      "v_cvt_pk_bf16_f32 v129, v200, v204\n"
      "v_mfma_f32_16x16x32_bf16 a[72:75], a[148:151], v[184:187], a[72:75]\n"
      "v_min_u32 v128, -2, v128\n"
      "v_min_u32 v129, -2, v129\n"
      "v_mfma_f32_16x16x32_bf16 a[76:79], a[148:151], v[188:191], a[76:79]\n"
      "s_mov_b32 s45, 0\n"
      "buffer_store_dwordx2 v[128:129], v22, s[76:79], s45 offen sc1\n"
      "v_mfma_f32_16x16x32_bf16 a[64:67], a[152:155], v[32:35], a[64:67]\n"
      "ds_read_b128 a[144:147], v230 offset:32\n"
      "v_cvt_pk_bf16_f32 v130, v193, v197\n"
      "v_cvt_pk_bf16_f32 v131, v201, v205\n"
      "v_mfma_f32_16x16x32_bf16 a[68:71], a[152:155], v[36:39], a[68:71]\n"
      "v_min_u32 v130, -2, v130\n"
      "v_min_u32 v131, -2, v131\n"
      "v_mfma_f32_16x16x32_bf16 a[72:75], a[152:155], v[40:43], a[72:75]\n"
      "s_mov_b32 s45, 4096\n"
      "buffer_store_dwordx2 v[130:131], v22, s[76:79], s45 offen sc1\n"
      "v_mfma_f32_16x16x32_bf16 a[76:79], a[152:155], v[44:47], a[76:79]\n"
      "v_cvt_pk_bf16_f32 v132, v194, v198\n"
      "v_cvt_pk_bf16_f32 v133, v202, v206\n"
      "v_mfma_f32_16x16x32_bf16 a[64:67], a[156:159], v[48:51], a[64:67]\n"
      "ds_read_b128 a[148:151], v230 offset:48\n"
      "v_min_u32 v132, -2, v132\n"
      "v_min_u32 v133, -2, v133\n"
      "v_mfma_f32_16x16x32_bf16 a[68:71], a[156:159], v[52:55], a[68:71]\n"
      "s_mov_b32 s45, 8192\n"
      "buffer_store_dwordx2 v[132:133], v22, s[76:79], s45 offen sc1\n"
      "v_mfma_f32_16x16x32_bf16 a[72:75], a[156:159], v[56:59], a[72:75]\n"
      "v_cvt_pk_bf16_f32 v134, v195, v199\n"
      "v_cvt_pk_bf16_f32 v135, v203, v207\n"
      "v_mfma_f32_16x16x32_bf16 a[76:79], a[156:159], v[60:63], a[76:79]\n"
      "v_min_u32 v134, -2, v134\n"
      "v_min_u32 v135, -2, v135\n"
      "v_mfma_f32_16x16x32_bf16 a[80:83], a[160:163], v[96:99], a[80:83]\n"
      "ds_read_b128 a[152:155], v238\n"
      "v_mfma_f32_16x16x32_bf16 a[84:87], a[160:163], v[100:103], a[84:87]\n"
      "ds_write_b128 v18, a[64:67] offset:0\n"
      "v_mfma_f32_16x16x32_bf16 a[88:91], a[160:163], v[104:107], a[88:91]\n"
      "s_mov_b32 s45, 12288\n"
      "buffer_store_dwordx2 v[134:135], v22, s[76:79], s45 offen sc1\n"
      "v_mfma_f32_16x16x32_bf16 a[92:95], a[160:163], v[108:111], a[92:95]\n"
      "v_accvgpr_read_b32 v192, a112\n"
      "v_accvgpr_read_b32 v193, a113\n"
      "v_mfma_f32_16x16x32_bf16 a[80:83], a[164:167], v[112:115], a[80:83]\n"
      "ds_read_b128 a[156:159], v238 offset:16\n"
      "v_accvgpr_read_b32 v194, a114\n"
      "v_accvgpr_read_b32 v195, a115\n"
      "v_mfma_f32_16x16x32_bf16 a[84:87], a[164:167], v[116:119], a[84:87]\n"
      "ds_write_b128 v18, a[68:71] offset:1024\n"
      "v_mfma_f32_16x16x32_bf16 a[88:91], a[164:167], v[120:123], a[88:91]\n"
      "v_accvgpr_read_b32 v196, a116\n"
      "v_accvgpr_read_b32 v197, a117\n"
      "v_mfma_f32_16x16x32_bf16 a[92:95], a[164:167], v[124:127], a[92:95]\n"
      "ds_write_b128 v18, a[72:75] offset:2048\n"
      "v_mfma_f32_16x16x32_bf16 a[80:83], a[168:171], v[64:67], a[80:83]\n"
      "ds_read_b128 a[160:163], v238 offset:32\n"
      "v_accvgpr_read_b32 v198, a118\n"
      "v_accvgpr_read_b32 v199, a119\n"
      "v_mfma_f32_16x16x32_bf16 a[84:87], a[168:171], v[68:71], a[84:87]\n"
      "v_accvgpr_read_b32 v200, a120\n"
      "v_accvgpr_read_b32 v201, a121\n"
      "v_mfma_f32_16x16x32_bf16 a[88:91], a[168:171], v[72:75], a[88:91]\n"
      "ds_write_b128 v18, a[76:79] offset:3072\n"
      "v_mfma_f32_16x16x32_bf16 a[92:95], a[168:171], v[76:79], a[92:95]\n"
      "v_accvgpr_read_b32 v202, a122\n"
      "v_accvgpr_read_b32 v203, a123\n"
      "s_waitcnt lgkmcnt(12)\n"
      "v_mfma_f32_16x16x32_bf16 a[80:83], a[172:175], v[80:83], a[80:83]\n"
      "ds_read_b128 a[164:167], v238 offset:48\n"
      "v_accvgpr_read_b32 v204, a124\n"
      "v_accvgpr_read_b32 v205, a125\n"
      "v_mfma_f32_16x16x32_bf16 a[84:87], a[172:175], v[84:87], a[84:87]\n"
      "v_accvgpr_read_b32 v206, a126\n"
      "v_accvgpr_read_b32 v207, a127\n"
      "v_mfma_f32_16x16x32_bf16 a[88:91], a[172:175], v[88:91], a[88:91]\n"
      "buffer_load_dwordx2 v[248:249], v23, s[76:79], 0 offen sc1\n"
      "v_mfma_f32_16x16x32_bf16 a[92:95], a[172:175], v[92:95], a[92:95]\n"
      "v_mfma_f32_16x16x32_bf16 a[80:83], a[176:179], v[160:163], a[80:83]\n"
      "ds_read_b128 a[168:171], v231\n"
      "v_mfma_f32_16x16x32_bf16 a[84:87], a[176:179], v[164:167], a[84:87]\n"
      "v_mfma_f32_16x16x32_bf16 a[88:91], a[176:179], v[168:171], a[88:91]\n"
      "v_mfma_f32_16x16x32_bf16 a[92:95], a[176:179], v[172:175], a[92:95]\n"
      "v_mfma_f32_16x16x32_bf16 a[80:83], a[180:183], v[176:179], a[80:83]\n"
      "ds_read_b128 a[172:175], v231 offset:16\n"
      "v_mfma_f32_16x16x32_bf16 a[84:87], a[180:183], v[180:183], a[84:87]\n"
      "v_mfma_f32_16x16x32_bf16 a[88:91], a[180:183], v[184:187], a[88:91]\n"
      "v_mfma_f32_16x16x32_bf16 a[92:95], a[180:183], v[188:191], a[92:95]\n"
      "v_mfma_f32_16x16x32_bf16 a[80:83], a[128:131], v[32:35], a[80:83]\n"
      "ds_read_b128 a[176:179], v231 offset:32\n"
      "v_mfma_f32_16x16x32_bf16 a[84:87], a[128:131], v[36:39], a[84:87]\n"
      "v_mfma_f32_16x16x32_bf16 a[88:91], a[128:131], v[40:43], a[88:91]\n"
      "v_mfma_f32_16x16x32_bf16 a[92:95], a[128:131], v[44:47], a[92:95]\n"
      "s_waitcnt lgkmcnt(12)\n"
      "v_mfma_f32_16x16x32_bf16 a[80:83], a[132:135], v[48:51], a[80:83]\n"
      "ds_read_b128 a[180:183], v231 offset:48\n"
      "v_mfma_f32_16x16x32_bf16 a[84:87], a[132:135], v[52:55], a[84:87]\n"
      "v_mfma_f32_16x16x32_bf16 a[88:91], a[132:135], v[56:59], a[88:91]\n"
      "v_mfma_f32_16x16x32_bf16 a[92:95], a[132:135], v[60:63], a[92:95]\n"
      "v_mfma_f32_16x16x32_bf16 a[96:99], a[136:139], v[96:99], a[96:99]\n"
      "ds_read_b128 a[128:131], v239\n"
      "v_mfma_f32_16x16x32_bf16 a[100:103], a[136:139], v[100:103], a[100:103]\n"
      "s_nop 0\n"
      "ds_write_b128 v19, a[80:83] offset:4096\n"
      "v_mfma_f32_16x16x32_bf16 a[104:107], a[136:139], v[104:107], a[104:107]\n"
      "v_mfma_f32_16x16x32_bf16 a[108:111], a[136:139], v[108:111], a[108:111]\n"
      "v_mfma_f32_16x16x32_bf16 a[96:99], a[140:143], v[112:115], a[96:99]\n"
      "ds_read_b128 a[132:135], v239 offset:16\n"
      "v_mfma_f32_16x16x32_bf16 a[100:103], a[140:143], v[116:119], a[100:103]\n"
      "ds_write_b128 v19, a[84:87] offset:5120\n"
      "v_mfma_f32_16x16x32_bf16 a[104:107], a[140:143], v[120:123], a[104:107]\n"
      "v_mfma_f32_16x16x32_bf16 a[108:111], a[140:143], v[124:127], a[108:111]\n"
      "ds_write_b128 v19, a[88:91] offset:6144\n"
      "v_mfma_f32_16x16x32_bf16 a[96:99], a[144:147], v[64:67], a[96:99]\n"
      "ds_read_b128 a[136:139], v239 offset:32\n"
      "v_mfma_f32_16x16x32_bf16 a[100:103], a[144:147], v[68:71], a[100:103]\n"
      "v_mfma_f32_16x16x32_bf16 a[104:107], a[144:147], v[72:75], a[104:107]\n"
      "ds_write_b128 v19, a[92:95] offset:7168\n"
      "v_mfma_f32_16x16x32_bf16 a[108:111], a[144:147], v[76:79], a[108:111]\n"
      "s_waitcnt lgkmcnt(13)\n"
      "v_mfma_f32_16x16x32_bf16 a[96:99], a[148:151], v[80:83], a[96:99]\n"
      "ds_read_b128 a[140:143], v239 offset:48\n"
      "v_mfma_f32_16x16x32_bf16 a[100:103], a[148:151], v[84:87], a[100:103]\n"
      "s_waitcnt lgkmcnt(1)\n"
      "s_barrier\n"
      "ds_read_b128 v[208:211], v21 offset:0\n"
      "ds_read_b128 v[212:215], v21 offset:1024\n"
      "v_mfma_f32_16x16x32_bf16 a[104:107], a[148:151], v[88:91], a[104:107]\n"
      "ds_read_b128 v[216:219], v21 offset:2048\n"
      "ds_read_b128 v[220:223], v21 offset:3072\n"
      "v_mfma_f32_16x16x32_bf16 a[108:111], a[148:151], v[92:95], a[108:111]\n"
      "ds_read_b128 v[128:131], v21 offset:4096\n"
      "ds_read_b128 v[132:135], v21 offset:5120\n"
      "v_mfma_f32_16x16x32_bf16 a[96:99], a[152:155], v[160:163], a[96:99]\n"
      "ds_read_b128 v[136:139], v21 offset:6144\n"
      "ds_read_b128 v[140:143], v21 offset:7168\n"
      "v_mfma_f32_16x16x32_bf16 a[100:103], a[152:155], v[164:167], a[100:103]\n"
      "v_mfma_f32_16x16x32_bf16 a[104:107], a[152:155], v[168:171], a[104:107]\n"
      "v_mfma_f32_16x16x32_bf16 a[108:111], a[152:155], v[172:175], a[108:111]\n"
      "v_mfma_f32_16x16x32_bf16 a[96:99], a[156:159], v[176:179], a[96:99]\n"
      "v_mfma_f32_16x16x32_bf16 a[100:103], a[156:159], v[180:183], a[100:103]\n"
      "s_waitcnt lgkmcnt(3)\n"
      "v_add_f32 v208, v208, v128\n"
      "v_add_f32 v209, v209, v129\n"
      "v_mfma_f32_16x16x32_bf16 a[104:107], a[156:159], v[184:187], a[104:107]\n"
      "v_add_f32 v210, v210, v130\n"
      "v_add_f32 v211, v211, v131\n"
      "v_mfma_f32_16x16x32_bf16 a[108:111], a[156:159], v[188:191], a[108:111]\n"
      "s_waitcnt lgkmcnt(2)\n"
      "v_add_f32 v212, v212, v132\n"
      "v_add_f32 v213, v213, v133\n"
      "v_mfma_f32_16x16x32_bf16 a[96:99], a[160:163], v[32:35], a[96:99]\n"
      "v_add_f32 v214, v214, v134\n"
      "v_add_f32 v215, v215, v135\n"
      "v_mfma_f32_16x16x32_bf16 a[100:103], a[160:163], v[36:39], a[100:103]\n"
      "s_waitcnt lgkmcnt(1)\n"
      "v_add_f32 v216, v216, v136\n"
      "v_add_f32 v217, v217, v137\n"
      "v_mfma_f32_16x16x32_bf16 a[104:107], a[160:163], v[40:43], a[104:107]\n"
      "v_add_f32 v218, v218, v138\n"
      "v_add_f32 v219, v219, v139\n"
      "v_mfma_f32_16x16x32_bf16 a[108:111], a[160:163], v[44:47], a[108:111]\n"
      "s_waitcnt lgkmcnt(0)\n"
      "v_add_f32 v220, v220, v140\n"
      "v_add_f32 v221, v221, v141\n"
      "v_mfma_f32_16x16x32_bf16 a[96:99], a[164:167], v[48:51], a[96:99]\n"
      "v_add_f32 v222, v222, v142\n"
      "v_add_f32 v223, v223, v143\n"
      "v_mfma_f32_16x16x32_bf16 a[100:103], a[164:167], v[52:55], a[100:103]\n"
      "v_mfma_f32_16x16x32_bf16 a[104:107], a[164:167], v[56:59], a[104:107]\n"
      "v_mfma_f32_16x16x32_bf16 a[108:111], a[164:167], v[60:63], a[108:111]\n"
      "v_mfma_f32_16x16x32_bf16 v[192:195], a[168:171], v[96:99], v[192:195]\n"
      "v_mfma_f32_16x16x32_bf16 v[196:199], a[168:171], v[100:103], v[196:199]\n"
      "s_nop 0\n"
      "ds_write_b128 v20, a[96:99] offset:8192\n"
      "v_mfma_f32_16x16x32_bf16 v[200:203], a[168:171], v[104:107], v[200:203]\n"
      "v_mfma_f32_16x16x32_bf16 v[204:207], a[168:171], v[108:111], v[204:207]\n"
      "v_mfma_f32_16x16x32_bf16 v[192:195], a[172:175], v[112:115], v[192:195]\n"
      "ds_write_b128 v20, a[100:103] offset:9216\n"
      "v_mfma_f32_16x16x32_bf16 v[196:199], a[172:175], v[116:119], v[196:199]\n"
      "v_mfma_f32_16x16x32_bf16 v[200:203], a[172:175], v[120:123], v[200:203]\n"
      "v_mfma_f32_16x16x32_bf16 v[204:207], a[172:175], v[124:127], v[204:207]\n"
      "ds_write_b128 v20, a[104:107] offset:10240\n"
      "v_mfma_f32_16x16x32_bf16 v[192:195], a[176:179], v[64:67], v[192:195]\n"
      "v_mfma_f32_16x16x32_bf16 v[196:199], a[176:179], v[68:71], v[196:199]\n"
      "v_mfma_f32_16x16x32_bf16 v[200:203], a[176:179], v[72:75], v[200:203]\n"
      "ds_write_b128 v20, a[108:111] offset:11264\n"
      "v_mfma_f32_16x16x32_bf16 v[204:207], a[176:179], v[76:79], v[204:207]\n"
      "v_mfma_f32_16x16x32_bf16 v[192:195], a[180:183], v[80:83], v[192:195]\n"
      "v_mfma_f32_16x16x32_bf16 v[196:199], a[180:183], v[84:87], v[196:199]\n"
      "s_waitcnt lgkmcnt(0)\n"
      "s_barrier\n"
      "ds_read_b128 v[144:147], v21 offset:8192\n"
      "ds_read_b128 v[148:151], v21 offset:9216\n"
      "v_mfma_f32_16x16x32_bf16 v[200:203], a[180:183], v[88:91], v[200:203]\n"
      "ds_read_b128 v[152:155], v21 offset:10240\n"
      "ds_read_b128 v[156:159], v21 offset:11264\n"
      "v_mfma_f32_16x16x32_bf16 v[204:207], a[180:183], v[92:95], v[204:207]\n"
      "v_mfma_f32_16x16x32_bf16 v[192:195], a[128:131], v[160:163], v[192:195]\n"
      "v_mfma_f32_16x16x32_bf16 v[196:199], a[128:131], v[164:167], v[196:199]\n"
      "v_mfma_f32_16x16x32_bf16 v[200:203], a[128:131], v[168:171], v[200:203]\n"
      "v_mfma_f32_16x16x32_bf16 v[204:207], a[128:131], v[172:175], v[204:207]\n"
      "s_waitcnt lgkmcnt(3)\n"
      "v_add_f32 v208, v208, v144\n"
      "v_add_f32 v209, v209, v145\n"
      "v_mfma_f32_16x16x32_bf16 v[192:195], a[132:135], v[176:179], v[192:195]\n"
      "v_add_f32 v210, v210, v146\n"
      "v_add_f32 v211, v211, v147\n"
      "v_mfma_f32_16x16x32_bf16 v[196:199], a[132:135], v[180:183], v[196:199]\n"
      "s_waitcnt lgkmcnt(2)\n"
      "v_add_f32 v212, v212, v148\n"
      "v_add_f32 v213, v213, v149\n"
      "v_mfma_f32_16x16x32_bf16 v[200:203], a[132:135], v[184:187], v[200:203]\n"
      "v_add_f32 v214, v214, v150\n"
      "v_add_f32 v215, v215, v151\n"
      "v_mfma_f32_16x16x32_bf16 v[204:207], a[132:135], v[188:191], v[204:207]\n"
      "s_waitcnt lgkmcnt(1)\n"
      "v_add_f32 v216, v216, v152\n"
      "v_add_f32 v217, v217, v153\n"
      "v_mfma_f32_16x16x32_bf16 v[192:195], a[136:139], v[32:35], v[192:195]\n"
      "v_add_f32 v218, v218, v154\n"
      "v_add_f32 v219, v219, v155\n"
      "v_mfma_f32_16x16x32_bf16 v[196:199], a[136:139], v[36:39], v[196:199]\n"
      "s_waitcnt lgkmcnt(0)\n"
      "v_add_f32 v220, v220, v156\n"
      "v_add_f32 v221, v221, v157\n"
      "v_mfma_f32_16x16x32_bf16 v[200:203], a[136:139], v[40:43], v[200:203]\n"
      "v_add_f32 v222, v222, v158\n"
      "v_add_f32 v223, v223, v159\n"
      "v_mfma_f32_16x16x32_bf16 v[204:207], a[136:139], v[44:47], v[204:207]\n"
      "v_mfma_f32_16x16x32_bf16 v[192:195], a[140:143], v[48:51], v[192:195]\n"
      "v_mfma_f32_16x16x32_bf16 v[196:199], a[140:143], v[52:55], v[196:199]\n"
      "v_mfma_f32_16x16x32_bf16 v[200:203], a[140:143], v[56:59], v[200:203]\n"
      "v_mfma_f32_16x16x32_bf16 v[204:207], a[140:143], v[60:63], v[204:207]\n"
      "s_nop 4\n"
      "v_pk_add_f32 v[192:193], v[192:193], v[208:209]\n"
      "v_pk_add_f32 v[196:197], v[196:197], v[212:213]\n"
      "v_pk_add_f32 v[200:201], v[200:201], v[216:217]\n"
      "v_pk_add_f32 v[204:205], v[204:205], v[220:221]\n"
      "v_pk_add_f32 v[194:195], v[194:195], v[210:211]\n"
      "v_pk_add_f32 v[198:199], v[198:199], v[214:215]\n"
      "v_pk_add_f32 v[202:203], v[202:203], v[218:219]\n"
      "v_pk_add_f32 v[206:207], v[206:207], v[222:223]\n"
      "v_cvt_pk_bf16_f32 v128, v192, v196\n"
      "v_cvt_pk_bf16_f32 v129, v200, v204\n"
      "v_min_u32 v128, -2, v128\n"
      "v_min_u32 v129, -2, v129\n"
      "s_mov_b32 s45, 262144\n"
      "buffer_store_dwordx2 v[128:129], v22, s[76:79], s45 offen sc1\n"
      "v_cvt_pk_bf16_f32 v130, v193, v197\n"
      "v_cvt_pk_bf16_f32 v131, v201, v205\n"
      "v_min_u32 v130, -2, v130\n"
      "v_min_u32 v131, -2, v131\n"
      "s_mov_b32 s45, 266240\n"
      "buffer_store_dwordx2 v[130:131], v22, s[76:79], s45 offen sc1\n"
      "v_cvt_pk_bf16_f32 v132, v194, v198\n"
      "v_cvt_pk_bf16_f32 v133, v202, v206\n"
      "v_min_u32 v132, -2, v132\n"
      "v_min_u32 v133, -2, v133\n"
      "s_mov_b32 s45, 270336\n"
      "buffer_store_dwordx2 v[132:133], v22, s[76:79], s45 offen sc1\n"
      "v_cvt_pk_bf16_f32 v134, v195, v199\n"
      "v_cvt_pk_bf16_f32 v135, v203, v207\n"
      "v_min_u32 v134, -2, v134\n"
      "v_min_u32 v135, -2, v135\n"
      "s_mov_b32 s45, 274432\n"
      "buffer_store_dwordx2 v[134:135], v22, s[76:79], s45 offen sc1\n"
      "s_waitcnt vmcnt(4)\n"
      :
        "+{v[24:31]}"(sm), "=&{v[248:251]}"(fo), "=&{a[248:255]}"(bw2), "+{s[64:79]}"(rs), "+{s[80:95]}"(sp)
      : "{v[8:23]}"(vin), "{s[96:99]}"(sq), "{s[36:39]}"(sr), "s"((__attribute__((address_space(3))) char*)smem)
      : "memory", "scc", "vcc",
        "v32", "v33", "v34", "v35", "v36", "v37", "v38", "v39", "v40", "v41", "v42", "v43", "v44", "v45", "v46", "v47",
        "v48", "v49", "v50", "v51", "v52", "v53", "v54", "v55", "v56", "v57", "v58", "v59", "v60", "v61", "v62", "v63",
        "v64", "v65", "v66", "v67", "v68", "v69", "v70", "v71", "v72", "v73", "v74", "v75", "v76", "v77", "v78", "v79",
        "v80", "v81", "v82", "v83", "v84", "v85", "v86", "v87", "v88", "v89", "v90", "v91", "v92", "v93", "v94", "v95",
        "v96", "v97", "v98", "v99", "v100", "v101", "v102", "v103", "v104", "v105", "v106", "v107", "v108", "v109", "v110", "v111",
        "v112", "v113", "v114", "v115", "v116", "v117", "v118", "v119", "v120", "v121", "v122", "v123", "v124", "v125", "v126", "v127",
        "v128", "v129", "v130", "v131", "v132", "v133", "v134", "v135", "v136", "v137", "v138", "v139", "v140", "v141", "v142", "v143",
        "v144", "v145", "v146", "v147", "v148", "v149", "v150", "v151", "v152", "v153", "v154", "v155", "v156", "v157", "v158", "v159",
        "v160", "v161", "v162", "v163", "v164", "v165", "v166", "v167", "v168", "v169", "v170", "v171", "v172", "v173", "v174", "v175",
        "v176", "v177", "v178", "v179", "v180", "v181", "v182", "v183", "v184", "v185", "v186", "v187", "v188", "v189", "v190", "v191",
        "v192", "v193", "v194", "v195", "v196", "v197", "v198", "v199", "v200", "v201", "v202", "v203", "v204", "v205", "v206", "v207",
        "v208", "v209", "v210", "v211", "v212", "v213", "v214", "v215", "v216", "v217", "v218", "v219", "v220", "v221", "v222", "v223",
        "v224", "v225", "v226", "v227", "v228", "v229", "v230", "v231", "v232", "v233", "v234", "v235", "v236", "v237", "v238", "v239",
        "v240", "v241", "v242", "v243", "v244", "v245", "v246", "v247", "v252", "v253", "v254", "v255", "a0", "a1", "a2", "a3",
        "a4", "a5", "a6", "a7", "a8", "a9", "a10", "a11", "a12", "a13", "a14", "a15", "a16", "a17", "a18", "a19",
        "a20", "a21", "a22", "a23", "a24", "a25", "a26", "a27", "a28", "a29", "a30", "a31", "a32", "a33", "a34", "a35",
        "a36", "a37", "a38", "a39", "a40", "a41", "a42", "a43", "a44", "a45", "a46", "a47", "a48", "a49", "a50", "a51",
        "a52", "a53", "a54", "a55", "a56", "a57", "a58", "a59", "a60", "a61", "a62", "a63", "a64", "a65", "a66", "a67",
        "a68", "a69", "a70", "a71", "a72", "a73", "a74", "a75", "a76", "a77", "a78", "a79", "a80", "a81", "a82", "a83",
        "a84", "a85", "a86", "a87", "a88", "a89", "a90", "a91", "a92", "a93", "a94", "a95", "a96", "a97", "a98", "a99",
        "a100", "a101", "a102", "a103", "a104", "a105", "a106", "a107", "a108", "a109", "a110", "a111", "a112", "a113", "a114", "a115",
        "a116", "a117", "a118", "a119", "a120", "a121", "a122", "a123", "a124", "a125", "a126", "a127", "a128", "a129", "a130", "a131",
        "a132", "a133", "a134", "a135", "a136", "a137", "a138", "a139", "a140", "a141", "a142", "a143", "a144", "a145", "a146", "a147",
        "a148", "a149", "a150", "a151", "a152", "a153", "a154", "a155", "a156", "a157", "a158", "a159", "a160", "a161", "a162", "a163",
        "a164", "a165", "a166", "a167", "a168", "a169", "a170", "a171", "a172", "a173", "a174", "a175", "a176", "a177", "a178", "a179",
        "a180", "a181", "a182", "a183", "a184", "a185", "a186", "a187", "a188", "a189", "a190", "a191", "a192", "a193", "a194", "a195",
        "a196", "a197", "a198", "a199", "a200", "a201", "a202", "a203", "a204", "a205", "a206", "a207", "a208", "a209", "a210", "a211",
        "a212", "a213", "a214", "a215", "a216", "a217", "a218", "a219", "a220", "a221", "a222", "a223", "a224", "a225", "a226", "a227",
        "a228", "a229", "a230", "a231", "a232", "a233", "a234", "a235", "a236", "s40", "s41", "s42", "s43", "s44", "s45", "s46",
        "s47");
  char* xb = (char*)((u64)rs[12] | ((u64)rs[13] << 32));
  gu16* outp = (gu16*)((u64)sp[14] | ((u64)sp[15] << 32));
  const u64 tag = (u64)fo[2] | ((u64)fo[3] << 32);
  if (__builtin_expect(!__all(fo[0] == fo[2] && fo[1] == fo[3]), 0)) {
#pragma unroll 1
    for (unsigned spins = 0; spins < (1u << 20); ++spins) {
      const u64 f = __hip_atomic_load((gu64*)(xb + vfl), __ATOMIC_RELAXED, __HIP_MEMORY_SCOPE_AGENT);
      if (__all(f == tag)) break;
      __builtin_amdgcn_s_sleep(1);
    }
    asm volatile("" ::: "memory");
  }
  const __amdgpu_buffer_rsrc_t rs_x = __builtin_amdgcn_make_buffer_rsrc((void*)xb, 0, X_BYTES, 0x00020000);
  int vo[4];
#pragma unroll
  for (int ss = 0; ss < 4; ++ss) vo[ss] = (prod * 4 + w) * 4096 + ((ss * 1024 + l * 16 + ROT * 128 * prod) & 4095);
  u32x4 p[4];
#pragma unroll
  for (int ss = 0; ss < 4; ++ss) p[ss] = __builtin_amdgcn_raw_buffer_load_b128(rs_x, vo[ss], 0, 16);

  float wide = c < kND ? lo2f(sm[1]) * lo2f(sm[2]) : 0.f;
  wide = row_sum(wide);
  wide = rbf(rbf(wide) + lo2f(sm[3]));
  float bb[8], w1[8], w0[8];
#pragma unroll
  for (int r = 0; r < 4; ++r) {
    bb[2 * r] = lo2f(bw2[r]); bb[2 * r + 1] = hi2f(bw2[r]);
    w1[2 * r] = lo2f(bw2[4 + r]); w1[2 * r + 1] = hi2f(bw2[4 + r]);
    w0[2 * r] = w1[2 * r] * 0.f; w0[2 * r + 1] = w1[2 * r + 1] * 0.f;
  }
#pragma unroll
  for (int e = 0; e < 8; ++e) asm volatile("" : "+v"(bb[e]), "+v"(w1[e]), "+v"(w0[e]));
  asm volatile("" : "+v"(wide));

#pragma unroll 1
  for (unsigned spins = 0; spins < (1u << 20); ++spins) {
    unsigned mx = 0;
#pragma unroll
    for (int ss = 0; ss < 4; ++ss) mx = max(max(mx, max(p[ss][0], p[ss][1])), max(p[ss][2], p[ss][3]));
    if (__all(mx != 0xffffffffu)) break;
    asm volatile("" ::: "memory");
#pragma unroll
    for (int ss = 0; ss < 4; ++ss) p[ss] = __builtin_amdgcn_raw_buffer_load_b128(rs_x, vo[ss], 0, 16);
  }
  float d0 = 0.f, d1 = 0.f;
#pragma unroll
  for (int r = 0; r < 4; ++r) {
    f32x2 x = (up(p[0][r]) + up(p[1][r])) + (up(p[2][r]) + up(p[3][r]));
    x = up(pk(x));
    x[0] += bb[2 * r]; x[1] += bb[2 * r + 1];
    x = up(pk(x));
    d0 = fmaf(x[0], x[0] > 0.f ? w1[2 * r] : w0[2 * r], d0);
    d1 = fmaf(x[1], x[1] > 0.f ? w1[2 * r + 1] : w0[2 * r + 1], d1);
  }
  float d = row_sum(d0 + d1);
  {
    const unsigned u = __float_as_uint(d);
    const auto q = __builtin_amdgcn_permlane16_swap(u, u, false, false);
    d = __uint_as_float(q[0]) + __uint_as_float(q[1]);
    const unsigned u2 = __float_as_uint(d);
    const auto q2 = __builtin_amdgcn_permlane32_swap(u2, u2, false, false);
    d = __uint_as_float(q2[0]) + __uint_as_float(q2[1]);
  }
  const float deep = rbf(rbf(d) + lo2f(sm[4]));
  const float z = rbf(wide + deep);
  const float y = __builtin_amdgcn_rcpf(1.f + __expf(-z));
  asm volatile(".Lwd_code_end:" :: "v"(y));
  if (l == 0) {
    outp[row] = __builtin_bit_cast(unsigned short, (__bf16)y);
    if (prod == 0 && w == 0)
      __hip_atomic_store((gu64*)(xb + X_EPOCH), tag, __ATOMIC_RELAXED, __HIP_MEMORY_SCOPE_AGENT);
  }
}

extern "C" void kernel_launch(void* const* d_in, const int* in_sizes, int n_in,
                              void* d_out, int out_size, void* d_ws, size_t ws_size,
                              hipStream_t stream) {
  (void)in_sizes; (void)n_in; (void)out_size; (void)ws_size;
  wd_fused<<<256, 256, 0, stream>>>(
      (const int*)d_in[0], (const unsigned short*)d_in[1], (const unsigned short*)d_in[2],
      (const unsigned short*)d_in[3], (const unsigned short*)d_in[4], (const unsigned short*)d_in[5],
      (const unsigned short*)d_in[6], (const unsigned short*)d_in[7], (const unsigned short*)d_in[8],
      (unsigned short*)d_out, (char*)d_ws);
}
```
